```python
import torch

def _softmax(x, axis=-1):
    return torch.softmax(x, dim=axis)
def _gelu(x):
    return torch.nn.functional.gelu(x, approximate="tanh")
def _silu(x):
    return torch.nn.functional.silu(x)
def _sigmoid(x):
    return torch.sigmoid(x)
def _layernorm(x, axis=-1, eps=1e-5):
    m = x.mean(dim=axis, keepdim=True)
    v = x.var(dim=axis, keepdim=True, unbiased=False)
    return (x - m) / torch.sqrt(v + eps)
def _scatter_add(tgt, idx, src, axis=0):
    return tgt.clone().index_add_(axis, idx, src)
def _segment_sum(src, seg, n):
    out = torch.zeros((n,) + src.shape[1:], dtype=src.dtype)
    return out.index_add_(0, seg, src)
def _pad(x, pw):
    flat = tuple(v for pair in reversed(pw) for v in pair)
    return torch.nn.functional.pad(x, flat)

kernel_name = 'sequential_preLN__ssm_conv_s4d__mixer_s_mlp__prompt'

def setup_inputs():

    _w = (lambda a: (lambda t: t.to(torch.bfloat16) if t.is_floating_point() else t)(torch.as_tensor(a)))
    def _A_setup():
        Bsz, L, D, N = 1, 2048, 512, 128
        return {
            "u": _w((lambda *s: torch.randn(*s, dtype=torch.bfloat16) * 0.05)(Bsz, L, D)),
            "log_A_real": _w((lambda *s: torch.randn(*s, dtype=torch.bfloat16) * 6.4)(D, N) * 0.5),
            "A_imag": _w((lambda *s: torch.randn(*s, dtype=torch.bfloat16) * 0.05)(D, N) * 3.14),
            "C_real": _w((lambda *s: torch.randn(*s, dtype=torch.bfloat16) * 0.05)(D, N) / N ** 0.5),
            "C_imag": _w((lambda *s: torch.randn(*s, dtype=torch.bfloat16) * 0.05)(D, N) / N ** 0.5),
            "log_dt": _w((lambda *s: torch.randn(*s, dtype=torch.bfloat16) * 0.05)(D) * 0.5 - 4.0),
            "D_skip": _w((lambda *s: torch.randn(*s, dtype=torch.bfloat16) * 0.05)(D)),
        }
    _A_in = _A_setup()

    def _B_setup():
        B, S, D, F = 1, 2048, 512, 1792
        return {
            "x":  _w((lambda *s: torch.randn(*s, dtype=torch.bfloat16) * 0.05)(B, S, D)),
            "w1": _w((lambda *s: torch.randn(*s, dtype=torch.bfloat16) * 6.4)(D, D) * (D ** -0.5)),
            "w2": _w((lambda *s: torch.randn(*s, dtype=torch.bfloat16) * 6.4)(D, D) * (D ** -0.5)),
            "w3": _w((lambda *s: torch.randn(*s, dtype=torch.bfloat16) * 6.4)(D, F) * (D ** -0.5)),
            "w4": _w((lambda *s: torch.randn(*s, dtype=torch.bfloat16) * 6.4)(F, D) * (F ** -0.5)),
        }
    _B_in = _B_setup()

    return {
        "A_u": _A_in["u"],
        "A_log_A_real": _A_in["log_A_real"],
        "A_A_imag": _A_in["A_imag"],
        "A_C_real": _A_in["C_real"],
        "A_C_imag": _A_in["C_imag"],
        "A_log_dt": _A_in["log_dt"],
        "A_D_skip": _A_in["D_skip"],
        "B_x": _B_in["x"],
        "B_w1": _B_in["w1"],
        "B_w2": _B_in["w2"],
        "B_w3": _B_in["w3"],
        "B_w4": _B_in["w4"],
    }

def reference(A_u, A_log_A_real, A_A_imag, A_C_real, A_C_imag, A_log_dt, A_D_skip, B_x, B_w1, B_w2, B_w3, B_w4):
    def _A_ref(u, log_A_real, A_imag, C_real, C_imag, log_dt, D_skip):
        L = u.shape[1]
        dt = torch.exp(log_dt)
        A = -torch.exp(log_A_real) + 1j * A_imag
        C = (C_real + 1j * C_imag) * dt[:, None]
        pos = (lambda x, dt: x.to(dt))(torch.arange(L), log_dt.dtype)
        P = torch.exp((A * dt[:, None])[:, :, None] * pos[None, None, :])
        K = 2.0 * torch.einsum("dn,dnl->dl", C, P).real
        L2 = 2 * L
        u_t = (lambda x, *d: x.permute(*d))(u, 0, 2, 1)
        Kf = (lambda x, *a, **k: torch.fft.rfft(x.float(), *a, **k))(K, L2)
        Uf = (lambda x, *a, **k: torch.fft.rfft(x.float(), *a, **k))(u_t, L2)
        y = (lambda x, *a, **k: torch.fft.irfft(x, *a, **k).to(torch.bfloat16))(Uf * Kf[None, :, :], L2)[:, :, :L]
        y = (lambda x, *d: x.permute(*d))(y, 0, 2, 1)
        return y + u * D_skip[None, None, :]

    def _B_ref(x, w1, w2, w3, w4):
        B, S, D = x.shape
        h = _gelu(_layernorm(x, axis=-1) @ w1)
        c = D // 4
        g0 = _pad(h[:, :, 0*c:1*c], ((0, 0), (1, 0), (0, 0)))[:, :S, :]
        g1 = _pad(h[:, :, 1*c:2*c], ((0, 0), (0, 1), (0, 0)))[:, 1:, :]
        g2 = _pad(h[:, :, 2*c:3*c], ((0, 0), (2, 0), (0, 0)))[:, :S, :]
        g3 = _pad(h[:, :, 3*c:   ], ((0, 0), (0, 2), (0, 0)))[:, 2:, :]
        hs = torch.concatenate([g0, g1, g2, g3], axis=-1) @ w2
        x1 = x + hs
        h2 = _gelu(_layernorm(x1, axis=-1) @ w3) @ w4
        return x1 + h2

    x = A_u
    h = x + _A_ref(_layernorm(x, axis=-1), A_log_A_real, A_A_imag, A_C_real, A_C_imag, A_log_dt, A_D_skip)
    h2 = h + _B_ref(_layernorm(h, axis=-1), B_w1, B_w2, B_w3, B_w4)
    return h2
```

```cpp
#include <hip/hip_runtime.h>
#include <hip/hip_bf16.h>
#include <stdint.h>

typedef unsigned short u16;
constexpr int L = 2048, D = 512, NS = 128, F = 1792;

__device__ __forceinline__ float bf2f(u16 v) { return __uint_as_float(((unsigned)v) << 16); }
__device__ __forceinline__ u16 f2bf(float f) { __hip_bfloat16 b = __float2bfloat16(f); return *reinterpret_cast<u16*>(&b); }
__device__ __forceinline__ float rbf(float f) { return bf2f(f2bf(f)); }

__device__ __forceinline__ float wave_sum(float v) {
  for (int o = 32; o > 0; o >>= 1) v += __shfl_xor(v, o);
  return v;
}

__global__ void ln_kernel(const u16* in, u16* out) {
  int row = blockIdx.x * 4 + (threadIdx.x >> 6), lane = threadIdx.x & 63;
  float v[8]; float s = 0;
  for (int j = 0; j < 8; ++j) { v[j] = bf2f(in[row * D + lane * 8 + j]); s += v[j]; }
  s = wave_sum(s); float mean = s * (1.f / D);
  float q = 0; for (int j = 0; j < 8; ++j) { float d = v[j] - mean; q += d * d; }
  q = wave_sum(q); float var = q * (1.f / D);
  float mb = rbf(mean), sb = rbf(sqrtf(rbf(rbf(var) + 1e-5f)));
  for (int j = 0; j < 8; ++j) out[row * D + lane * 8 + j] = f2bf(rbf(v[j] - mb) / sb);
}

__global__ void kgen_kernel(const u16* logA, const u16* Aim, const u16* Cr, const u16* Ci, const u16* logdt, float* K) {
  int d = blockIdx.y; int l = blockIdx.x * 256 + threadIdx.x;
  float dt = rbf(expf(bf2f(logdt[d])));
  float pos = rbf((float)l);
  float acc = 0;
  for (int n = 0; n < NS; ++n) {
    float ar = -rbf(expf(bf2f(logA[d * NS + n]))) * dt, ai = bf2f(Aim[d * NS + n]) * dt;
    float cr = bf2f(Cr[d * NS + n]) * dt, ci = bf2f(Ci[d * NS + n]) * dt;
    float e = expf(ar * pos); float sn, cs; sincosf(ai * pos, &sn, &cs);
    acc += cr * e * cs - ci * e * sn;
  }
  K[d * L + l] = 2.f * acc;
}

__global__ void conv_kernel(const u16* x, const u16* u, const float* K, const u16* Dskip, u16* h) {
  int d = blockIdx.x * 64 + (threadIdx.x & 63); int l = blockIdx.y * 4 + (threadIdx.x >> 6);
  float acc = 0;
  for (int j = 0; j <= l; ++j) acc += K[d * L + j] * bf2f(u[(l - j) * D + d]);
  float y = rbf(acc);
  float t = rbf(y + rbf(bf2f(u[l * D + d]) * bf2f(Dskip[d])));
  h[l * D + d] = f2bf(bf2f(x[l * D + d]) + t);
}

template <int MODE>
__global__ void gemm_kernel(const u16* A, const u16* W, u16* out, const u16* r1, const u16* r2, int K, int N) {
  int n = blockIdx.x * 64 + (threadIdx.x & 63); int m = blockIdx.y * 4 + (threadIdx.x >> 6);
  float acc = 0;
  for (int k = 0; k < K; ++k) {
    int mm = m;
    if (MODE == 2) { int g = k >> 7; mm = m + (g == 0 ? -1 : g == 1 ? 1 : g == 2 ? -2 : 2); }
    float a = (mm >= 0 && mm < L) ? bf2f(A[mm * K + k]) : 0.f;
    acc += a * bf2f(W[k * N + n]);
  }
  float v = rbf(acc);
  if (MODE == 1 || MODE == 3) { float t = 0.7978845608f * (v + 0.044715f * v * v * v); v = 0.5f * v * (1.f + tanhf(t)); out[m * N + n] = f2bf(v); }
  if (MODE == 2) out[m * N + n] = f2bf(bf2f(r1[m * N + n]) + v);
  if (MODE == 4) { float t = rbf(bf2f(r1[m * N + n]) + v); out[m * N + n] = f2bf(bf2f(r2[m * N + n]) + t); }
}

extern "C" void kernel_launch(void* const* d_in, const int* in_sizes, int n_in, void* d_out, int out_size, void* d_ws, size_t ws_size, hipStream_t stream) {
  const u16* x = (const u16*)d_in[0];
  char* ws = (char*)d_ws;
  u16* u = (u16*)(ws); u16* h = (u16*)(ws + (2 << 20)); u16* xb = (u16*)(ws + (4 << 20)); u16* xbb = (u16*)(ws + (6 << 20));
  u16* h1 = (u16*)(ws + (8 << 20)); u16* x1 = (u16*)(ws + (10 << 20)); u16* x1n = (u16*)(ws + (12 << 20));
  u16* H = (u16*)(ws + (16 << 20)); float* K = (float*)(ws + (32 << 20));
  ln_kernel<<<L / 4, 256, 0, stream>>>(x, u);
  kgen_kernel<<<dim3(L / 256, D), 256, 0, stream>>>((const u16*)d_in[1], (const u16*)d_in[2], (const u16*)d_in[3], (const u16*)d_in[4], (const u16*)d_in[5], K);
  conv_kernel<<<dim3(D / 64, L / 4), 256, 0, stream>>>(x, u, K, (const u16*)d_in[6], h);
  ln_kernel<<<L / 4, 256, 0, stream>>>(h, xb);
  ln_kernel<<<L / 4, 256, 0, stream>>>(xb, xbb);
  gemm_kernel<1><<<dim3(D / 64, L / 4), 256, 0, stream>>>(xbb, (const u16*)d_in[8], h1, nullptr, nullptr, D, D);
  gemm_kernel<2><<<dim3(D / 64, L / 4), 256, 0, stream>>>(h1, (const u16*)d_in[9], x1, xb, nullptr, D, D);
  ln_kernel<<<L / 4, 256, 0, stream>>>(x1, x1n);
  gemm_kernel<3><<<dim3(F / 64, L / 4), 256, 0, stream>>>(x1n, (const u16*)d_in[10], H, nullptr, nullptr, D, F);
  gemm_kernel<4><<<dim3(D / 64, L / 4), 256, 0, stream>>>(H, (const u16*)d_in[11], (u16*)d_out, x1, h, F, D);
}
```

```cpp
#include <hip/hip_runtime.h>
#include <hip/hip_bf16.h>
#include <stdint.h>

typedef unsigned short u16;
typedef unsigned int u32;
typedef u32 u32x2 __attribute__((ext_vector_type(2)));
typedef u32 u32x4 __attribute__((ext_vector_type(4)));
typedef float f32x2 __attribute__((ext_vector_type(2)));
typedef float f32x4 __attribute__((ext_vector_type(4)));
typedef float f32x16 __attribute__((ext_vector_type(16)));
typedef short s16x4 __attribute__((ext_vector_type(4)));
typedef short s16x8 __attribute__((ext_vector_type(8)));
typedef __bf16 bf16x2 __attribute__((ext_vector_type(2)));
typedef __bf16 bf16x8 __attribute__((ext_vector_type(8)));
#define LAS __attribute__((address_space(3)))
#define GAS __attribute__((address_space(1)))
#define DI __device__ __forceinline__

constexpr int L = 2048, D = 512, NS = 128, F = 1792;
constexpr int KR_LEN = 2112;
constexpr int KR_OFF = 2063;

constexpr size_t WS_UT = 0;
constexpr size_t WS_XT = 2u << 20;
constexpr size_t WS_KR = 4u << 20;
constexpr size_t WS_HP = 7u << 20;
constexpr size_t WS_HF = 9u << 20;
constexpr size_t WS_XB = 11u << 20;
constexpr size_t WS_H1 = 13u << 20;
constexpr size_t WS_X1 = 16u << 20;
constexpr size_t WS_ST = 18u << 20;
constexpr size_t WS_CS = 19u << 20;
constexpr size_t WS_HH = 20u << 20;

DI float bf2f(u32 v) { return __uint_as_float(v << 16); }
DI float bflo(u32 v) { return __uint_as_float(v << 16); }
DI float bfhi(u32 v) { return __uint_as_float(v & 0xffff0000u); }
DI u32 pk2(float lo, float hi) { f32x2 v = {lo, hi}; bf16x2 p = __builtin_convertvector(v, bf16x2); return __builtin_bit_cast(u32, p); }
DI float rbf(float f) { return bflo(pk2(f, 0.f) & 0xffffu); }

DI float dpp_add(float v, float acc_src, int) { return v; }
template <int CTRL, int RMASK>
DI float dppmov(float v) { return __int_as_float(__builtin_amdgcn_update_dpp(0, __float_as_int(v), CTRL, RMASK, 0xf, false)); }
DI float wave_sum(float v) {
  v += dppmov<0x111, 0xf>(v);
  v += dppmov<0x112, 0xf>(v);
  v += dppmov<0x114, 0xf>(v);
  v += dppmov<0x118, 0xf>(v);
  v += dppmov<0x142, 0xa>(v);
  v += dppmov<0x143, 0xc>(v);
  return __int_as_float(__builtin_amdgcn_readlane(__float_as_int(v), 63));
}
DI float swap32_add(float v) {
  auto r = __builtin_amdgcn_permlane32_swap(__float_as_uint(v), __float_as_uint(v), false, false);
  return __uint_as_float(r[0]) + __uint_as_float(r[1]);
}
DI float gelu_f(float z) {
  const float c0 = -2.f * 0.7978845608028654f * 1.4426950408889634f, c1 = c0 * 0.044715f;
  float a = z * __builtin_fmaf(z * z, c1, c0);
  float e = __builtin_amdgcn_exp2f(a);
  return z * __builtin_amdgcn_rcpf(1.f + e);
}
DI void glds16(const void* g, u32 lds_off, char* smem) {
  __builtin_amdgcn_global_load_lds((const GAS u32*)g, (LAS u32*)(smem + lds_off), 16, 0, 0);
}
#define WAITV(n) asm volatile("s_waitcnt vmcnt(" #n ")" ::: "memory")
#define BAR() do { asm volatile("s_waitcnt lgkmcnt(0)" ::: "memory"); __builtin_amdgcn_s_barrier(); asm volatile("" ::: "memory"); } while (0)
DI bf16x8 tr_frag(char* smem, u32 a0, u32 a1) {
  s16x4 a = __builtin_amdgcn_ds_read_tr16_b64_v4i16((LAS s16x4*)(smem + a0));
  s16x4 b = __builtin_amdgcn_ds_read_tr16_b64_v4i16((LAS s16x4*)(smem + a1));
  return __builtin_bit_cast(bf16x8, __builtin_shufflevector(a, b, 0, 1, 2, 3, 4, 5, 6, 7));
}
DI f32x16 mfma32(bf16x8 a, bf16x8 b, f32x16 c) { return __builtin_amdgcn_mfma_f32_32x32x16_bf16(a, b, c, 0, 0, 0); }

struct KTab { u16 v[KR_LEN]; };
constexpr int bf16_round_pos(int t) {
  if (t < 256) return t;
  int sh = t < 512 ? 1 : t < 1024 ? 2 : 3;
  int q = t >> sh, r = t & ((1 << sh) - 1), half = 1 << (sh - 1);
  if (r > half || (r == half && (q & 1))) ++q;
  return q << sh;
}
constexpr KTab make_ktab() {
  KTab t{};
  for (int i = 0; i < KR_LEN; ++i) {
    int tau = KR_OFF - i; int idx = 1024;
    if (tau >= 0 && tau < L) {
      int p = bf16_round_pos(tau); int m = 0, col = 0;
      if (p < 256) { m = p & 7; col = p >> 3; }
      else if (p < 512) { m = 8 + ((p & 7) >> 1); col = (p - 256) >> 3; }
      else if (p < 1024) { m = 12 + 2 * ((p - 512) >> 8) + ((p >> 2) & 1); col = (p & 255) >> 3; }
      else { m = 16 + ((p - 1024) >> 8); col = (p & 255) >> 3; }
      idx = m * 32 + col;
    }
    t.v[i] = (u16)idx;
  }
  return t;
}
__device__ const KTab g_ktab = make_ktab();

struct cpx { float r, i; };
DI cpx cmul(cpx a, cpx b) { return {a.r * b.r - a.i * b.i, a.r * b.i + a.i * b.r}; }
DI cpx cexp_p(float ar, float ai_rev, float p) {
  float e = __builtin_amdgcn_exp2f(ar * p * 1.4426950408889634f);
  float rv = ai_rev * p; rv -= floorf(rv);
  return {e * __builtin_amdgcn_cosf(rv), e * __builtin_amdgcn_sinf(rv)};
}

constexpr int KA_LN = 128, KA_KG = 256, KA_CS = 36, KA_Z = 4;
constexpr int KA_GRID = KA_LN + KA_KG + KA_CS + KA_Z;
constexpr int KA_LDS = 64 * 1024;

__global__ void __launch_bounds__(256) kA(const u16* __restrict__ x, const u16* __restrict__ logA, const u16* __restrict__ Aim,
                                          const u16* __restrict__ Cr, const u16* __restrict__ Ci, const u16* __restrict__ logdt,
                                          const u16* __restrict__ w1, const u16* __restrict__ w3, char* __restrict__ ws) {
  extern __shared__ __attribute__((aligned(16))) char smem[];
  const int tid = threadIdx.x, lane = tid & 63, w = __builtin_amdgcn_readfirstlane(tid >> 6);
  int b = blockIdx.x;
  if (b < KA_LN) {
    const int xc = b & 7, a = b >> 3; const int g = (a >> 2) * 8 + xc; const int tb = 4 * g + (a & 3);
    const int t0 = tb * 16 + w * 4;
    u32x4 xv[4];
#pragma unroll
    for (int r = 0; r < 4; ++r) xv[r] = *(const u32x4*)(x + (size_t)(t0 + r) * D + lane * 8);
    float uo[4][8];
#pragma unroll
    for (int r = 0; r < 4; ++r) {
      float v[8];
#pragma unroll
      for (int j = 0; j < 4; ++j) { v[2 * j] = bflo(xv[r][j]); v[2 * j + 1] = bfhi(xv[r][j]); }
      float s = 0;
#pragma unroll
      for (int j = 0; j < 8; ++j) s += v[j];
      float mean = wave_sum(s) * (1.f / D);
      float q = 0;
#pragma unroll
      for (int j = 0; j < 8; ++j) { float d = v[j] - mean; q += d * d; }
      float var = wave_sum(q) * (1.f / D);
      float mb = rbf(mean), sb = rbf(sqrtf(rbf(rbf(var) + 1e-5f)));
      float rs = 1.f / sb;
#pragma unroll
      for (int j = 0; j < 8; ++j) uo[r][j] = rbf(v[j] - mb) * rs;
    }
#pragma unroll
    for (int j = 0; j < 8; ++j) {
      int ch = lane * 8 + j; int sw = lane & 7;
      u32x2 uu = {pk2(uo[0][j], uo[1][j]), pk2(uo[2][j], uo[3][j])};
      u32 x0 = xv[0][j >> 1], x1 = xv[1][j >> 1], x2 = xv[2][j >> 1], x3 = xv[3][j >> 1];
      u32x2 xx;
      if (j & 1) { xx[0] = (x0 >> 16) | (x1 & 0xffff0000u); xx[1] = (x2 >> 16) | (x3 & 0xffff0000u); }
      else { xx[0] = (x0 & 0xffffu) | (x1 << 16); xx[1] = (x2 & 0xffffu) | (x3 << 16); }
      *(u32x2*)(smem + ch * 64 + ((w ^ sw) * 8)) = uu;
      *(u32x2*)(smem + ch * 64 + (((4 + w) ^ sw) * 8)) = xx;
    }
    __syncthreads();
#pragma unroll
    for (int it = 0; it < 8; ++it) {
      int unit = it * 256 + tid; int arr = unit >> 10, ch = (unit >> 1) & 511, half = unit & 1;
      int sw = (ch >> 3) & 7; int s0 = arr * 4 + half * 2;
      u32x2 p0 = *(u32x2*)(smem + ch * 64 + ((s0 ^ sw) * 8));
      u32x2 p1 = *(u32x2*)(smem + ch * 64 + (((s0 + 1) ^ sw) * 8));
      u32x4 o = {p0[0], p0[1], p1[0], p1[1]};
      char* dst = ws + (arr ? WS_XT : WS_UT) + (size_t)ch * (L * 2) + (tb * 16 + half * 8) * 2;
      *(u32x4*)dst = o;
    }
    return;
  }
  b -= KA_LN;
  if (b < KA_KG) {
    const int ch = 2 * b + (w >> 1); const int n = 64 * (w & 1) + lane;
    const float dt = rbf(expf(bf2f(logdt[ch])));
    const float ar = -rbf(expf(bf2f(logA[ch * NS + n]))) * dt;
    const float ai = bf2f(Aim[ch * NS + n]) * dt * 0.15915494309189535f;
    const cpx c = {2.f * bf2f(Cr[ch * NS + n]) * dt, 2.f * bf2f(Ci[ch * NS + n]) * dt};
    char* tb = smem + w * (53 * 272);
    char* rowB = tb + lane * 4;
    char* rowA = tb + 32 * 272 + lane * 4;
    cpx w1c = cexp_p(ar, ai, 1.f), w8 = cexp_p(ar, ai, 8.f), e256 = cexp_p(ar, ai, 256.f);
    {
      cpx e = {1.f, 0.f};
#pragma unroll
      for (int col = 0; col < 32; ++col) { *(u32*)(rowB + col * 272) = pk2(e.r, e.i); e = cmul(e, w8); }
    }
    {
      cpx e = c;
#pragma unroll
      for (int m = 0; m < 8; ++m) { *(u32*)(rowA + m * 272) = pk2(e.r, -e.i); e = cmul(e, w1c); }
      cpx w2 = cmul(w1c, w1c), w4 = cmul(w2, w2);
      cpx c256 = cmul(c, e256); e = c256;
#pragma unroll
      for (int m = 8; m < 12; ++m) { *(u32*)(rowA + m * 272) = pk2(e.r, -e.i); e = cmul(e, w2); }
      cpx c512 = cmul(c256, e256), c768 = cmul(c512, e256);
      *(u32*)(rowA + 12 * 272) = pk2(c512.r, -c512.i); e = cmul(c512, w4); *(u32*)(rowA + 13 * 272) = pk2(e.r, -e.i);
      *(u32*)(rowA + 14 * 272) = pk2(c768.r, -c768.i); e = cmul(c768, w4); *(u32*)(rowA + 15 * 272) = pk2(e.r, -e.i);
      e = cmul(c768, e256);
#pragma unroll
      for (int m = 16; m < 21; ++m) { *(u32*)(rowA + m * 272) = pk2(e.r, -e.i); e = cmul(e, e256); }
    }
    f32x16 acc = {};
    {
      const int m = lane & 31, h = lane >> 5;
#pragma unroll
      for (int s = 0; s < 8; ++s) {
        bf16x8 af = *(const bf16x8*)(tb + (32 + m) * 272 + 32 * s + 16 * h);
        bf16x8 bf = *(const bf16x8*)(tb + m * 272 + 32 * s + 16 * h);
        acc = mfma32(af, bf, acc);
      }
    }
    __syncthreads();
    {
      float* kv = (float*)(smem + w * 4096);
      const int col = lane & 31, h = lane >> 5;
#pragma unroll
      for (int r = 0; r < 16; ++r) kv[((r & 3) + 8 * (r >> 2) + 4 * h) * 32 + col] = acc[r];
    }
    __syncthreads();
    {
      for (int i = tid; i < 2048; i += 256) {
        int chl = i >> 10, e = i & 1023;
        float v = ((float*)(smem + (2 * chl) * 4096))[e] + ((float*)(smem + (2 * chl + 1) * 4096))[e];
        ((float*)(smem + 16384 + chl * 4224))[e] = v;
      }
      if (tid < 2) ((float*)(smem + 16384 + tid * 4224))[1024] = 0.f;
    }
    __syncthreads();
    for (int unit = tid; unit < 2 * (KR_LEN / 8); unit += 256) {
      int chl = unit >= KR_LEN / 8; int u8 = unit - chl * (KR_LEN / 8);
      u32x4 id = *(const u32x4*)(g_ktab.v + u8 * 8);
      const float* kv = (const float*)(smem + 16384 + chl * 4224);
      u32x4 o;
#pragma unroll
      for (int j = 0; j < 4; ++j) o[j] = pk2(kv[id[j] & 0xffffu], kv[id[j] >> 16]);
      *(u32x4*)(ws + WS_KR + ((size_t)(2 * b + chl) * KR_LEN + u8 * 8) * 2) = o;
    }
    return;
  }
  b -= KA_KG;
  if (b < KA_CS) {
    const u16* W; int N, n0; float* dst;
    if (b < 8) { W = w1; N = D; n0 = b * 64; dst = (float*)(ws + WS_CS) + n0; }
    else { W = w3; N = F; n0 = (b - 8) * 64; dst = (float*)(ws + WS_CS) + D + n0; }
    const int cp = tid & 7, rg = tid >> 3;
    float s[8] = {};
#pragma unroll
    for (int r = 0; r < 16; ++r) {
      u32x4 v = *(const u32x4*)(W + (size_t)(rg * 16 + r) * N + n0 + cp * 8);
#pragma unroll
      for (int j = 0; j < 4; ++j) { s[2 * j] += bflo(v[j]); s[2 * j + 1] += bfhi(v[j]); }
    }
    float* red = (float*)smem;
#pragma unroll
    for (int j = 0; j < 8; ++j) red[rg * 64 + cp * 8 + j] = s[j];
    __syncthreads();
    if (tid < 64) { float t = 0; for (int r = 0; r < 32; ++r) t += red[r * 64 + tid]; dst[tid] = t; }
    return;
  }
  b -= KA_CS;
  {
    char* dst = ws + WS_H1 + (b >> 1 ? (size_t)65 * 32 * 1024 : 0) + (b & 1) * 16384;
    u32x4 z = {0, 0, 0, 0};
#pragma unroll
    for (int i = 0; i < 4; ++i) *(u32x4*)(dst + i * 4096 + tid * 16) = z;
  }
}

constexpr int KC_STR = 4416;
constexpr int KC_CH = 4 * KC_STR;
constexpr int U_QS = 1568;
constexpr int U_CH = 4 * U_QS;
constexpr int KB_U0 = 2 * KC_CH;
constexpr int KB_RED = KB_U0 + 2 * U_CH;
constexpr int KB_LDS = KB_RED + 16384;

__global__ void __launch_bounds__(256) kB(const u16* __restrict__ dskip, char* __restrict__ ws) {
  extern __shared__ __attribute__((aligned(16))) char smem[];
  const int tid = threadIdx.x, lane = tid & 63, w = __builtin_amdgcn_readfirstlane(tid >> 6);
  const int b = blockIdx.x;
  const int chl = w >> 1, ih = w & 1, ch = 2 * b + chl;
  const int n = lane & 31, h = lane >> 5;
  u32x2 xq[4];
  {
    const char* xp = ws + WS_XT + (size_t)ch * (L * 2) + (1024 * ih + 32 * n + 4 * h) * 2;
#pragma unroll
    for (int q = 0; q < 4; ++q) xq[q] = *(const u32x2*)(xp + 16 * q);
  }
  const float dsk = bf2f(dskip[ch]);
  u32x4 uv[2];
#pragma unroll
  for (int c = 0; c < 2; ++c) uv[c] = *(const u32x4*)(ws + WS_UT + (size_t)(2 * b + c) * (L * 2) + tid * 16);
  u32x4 k0[3]; u32x2 k1[3];
#pragma unroll
  for (int it = 0; it < 3; ++it) {
    int unit = it * 256 + tid;
    if (unit < 2 * (KR_LEN / 8)) {
      int c = unit >= KR_LEN / 8; int u8 = unit - c * (KR_LEN / 8);
      const char* src = ws + WS_KR + ((size_t)(2 * b + c) * KR_LEN + u8 * 8) * 2;
      k0[it] = *(const u32x4*)src; k1[it] = *(const u32x2*)(src + 16);
    }
  }
  { int c = tid >> 7, q = (tid >> 5) & 3, cc = tid & 31; u32x4 z = {0, 0, 0, 0};
    *(u32x4*)(smem + KB_U0 + c * U_CH + q * U_QS + cc * 16) = z; }
#pragma unroll
  for (int c = 0; c < 2; ++c) *(u32x4*)(smem + KB_U0 + c * U_CH + (tid & 3) * U_QS + (32 + (tid >> 2)) * 16) = uv[c];
#pragma unroll
  for (int it = 0; it < 3; ++it) {
    int unit = it * 256 + tid;
    if (unit < 2 * (KR_LEN / 8)) {
      int c = unit >= KR_LEN / 8; int u8 = unit - c * (KR_LEN / 8);
      char* dst = smem + c * KC_CH + u8 * 16;
      u32 d0 = k0[it][0], d1 = k0[it][1], d2 = k0[it][2], d3 = k0[it][3], d4 = k1[it][0], d5 = k1[it][1];
      u32 a10 = __builtin_amdgcn_alignbyte(d1, d0, 2), a21 = __builtin_amdgcn_alignbyte(d2, d1, 2), a32 = __builtin_amdgcn_alignbyte(d3, d2, 2),
          a43 = __builtin_amdgcn_alignbyte(d4, d3, 2), a54 = __builtin_amdgcn_alignbyte(d5, d4, 2);
      *(u32x4*)(dst + 0 * KC_STR) = (u32x4){d0, d1, d2, d3};
      *(u32x4*)(dst + 1 * KC_STR) = (u32x4){a10, a21, a32, a43};
      *(u32x4*)(dst + 2 * KC_STR) = (u32x4){d1, d2, d3, d4};
      *(u32x4*)(dst + 3 * KC_STR) = (u32x4){a21, a32, a43, a54};
    }
  }
  __syncthreads();
  f32x16 acc0 = {}, acc1 = {};
  {
    const int m = n;
    const char* abase = smem + chl * KC_CH + ((3 - m) & 3) * KC_STR + 8 * (515 + 2 * h - (m >> 2)) - 32 * (32 * ih - 1);
    const char* ubase = smem + KB_U0 + chl * U_CH + h * U_QS + (32 + n - 16 * ih) * 16;
#pragma unroll
    for (int ii = 0; ii < 32; ++ii) {
      u32 o1 = 0, o2 = 0;
      const char* pa1 = abase - 32 * ii; const char* pa2 = abase - 32 * (ii + 64);
      u32x2 a1l = *(const u32x2*)pa1, a2l = *(const u32x2*)pa2;
      const char* pa1h = pa1 + 8; const char* pa2h = pa2 + 8;
      asm volatile("" : "+v"(pa1h), "+v"(pa2h));
      u32x2 a1h = *(const u32x2*)pa1h, a2h = *(const u32x2*)pa2h;
      (void)o1; (void)o2;
      const char* pb = (ii & 1) ? (ubase - ((ii - 1) / 2) * 16) : (ubase + 2 * U_QS - (ii / 2) * 16);
      bf16x8 b0 = *(const bf16x8*)pb, b1 = *(const bf16x8*)(pb + 32 * 16);
      bf16x8 A1 = __builtin_bit_cast(bf16x8, (u32x4){a1l[0], a1l[1], a1h[0], a1h[1]});
      bf16x8 A2 = __builtin_bit_cast(bf16x8, (u32x4){a2l[0], a2l[1], a2h[0], a2h[1]});
      acc0 = mfma32(A1, b0, acc0);
      acc1 = mfma32(A1, b1, acc1);
      acc1 = mfma32(A2, b0, acc1);
    }
  }
  {
    f32x16 snd = ih ? acc0 : acc1;
    char* dst = smem + KB_RED + w * 4096 + lane * 16;
#pragma unroll
    for (int q = 0; q < 4; ++q) *(f32x4*)(dst + q * 1024) = (f32x4){snd[4 * q], snd[4 * q + 1], snd[4 * q + 2], snd[4 * q + 3]};
  }
  __syncthreads();
  {
    f32x16 y = ih ? acc1 : acc0;
    const char* src = smem + KB_RED + (w ^ 1) * 4096 + lane * 16;
    char* hp = ws + WS_HP + (size_t)(16 * ih + (n >> 1)) * 65536 + ch * 128 + (32 * (n & 1) + 4 * h) * 2;
#pragma unroll
    for (int q = 0; q < 4; ++q) {
      f32x4 p = *(const f32x4*)(src + q * 1024);
      u32x2 uu = *(const u32x2*)(smem + KB_U0 + chl * U_CH + q * U_QS + (32 + 32 * ih + n) * 16 + 8 * h);
      float o[4];
#pragma unroll
      for (int j = 0; j < 4; ++j) {
        float yv = rbf(y[4 * q + j] + p[j]);
        float uval = (j & 1) ? bfhi(uu[j >> 1]) : bflo(uu[j >> 1]);
        float xval = (j & 1) ? bfhi(xq[q][j >> 1]) : bflo(xq[q][j >> 1]);
        o[j] = xval + rbf(yv + rbf(uval * dsk));
      }
      *(u32x2*)(hp + 16 * q) = (u32x2){pk2(o[0], o[1]), pk2(o[2], o[3])};
    }
  }
}

DI u32 img128_srcoff(int lane) { return ((((lane >> 2) & 1) ^ ((lane >> 4) & 1)) * 64) + (lane & 3) * 16; }
DI u32 img128_traddr(int lane, int ct) {
  int h = lane >> 5, blk = (lane >> 4) & 1, q = (lane & 15) >> 2, p = lane & 3;
  int row = 4 * h + q;
  return row * 128 + ((ct ^ ((q >> 1) & 1)) * 64) + blk * 32 + p * 8;
}

constexpr int KC_LDS = 128 * 1024 + 1024;
__global__ void __launch_bounds__(256) kC(const u16* __restrict__ w1, char* __restrict__ ws) {
  extern __shared__ __attribute__((aligned(16))) char smem[];
  const int tid = threadIdx.x, lane = tid & 63, w = __builtin_amdgcn_readfirstlane(tid >> 6);
  const int b = blockIdx.x; const int panel = (b & 7) * 4 + ((b >> 3) & 3), ct = b >> 5;
  const int kh = w >> 1, nh = w & 1; const int h = lane >> 5;
  f32x4 cs[4];
  { const float* c = (const float*)(ws + WS_CS) + ct * 64 + nh * 32 + 4 * h;
#pragma unroll
    for (int q = 0; q < 4; ++q) cs[q] = *(const f32x4*)(c + 8 * q); }
  {
    const int img = w & 1, half = w >> 1;
    const char* src = img ? ((const char*)w1 + ct * 128) : (ws + WS_HP + (size_t)panel * 65536);
    const int pitch = img ? 1024 : 128;
    const char* sl = src + (size_t)(half * 256 + (lane >> 3)) * pitch + img128_srcoff(lane);
#pragma unroll
    for (int i = 0; i < 32; ++i) glds16(sl + (size_t)i * 8 * pitch, img * 65536 + (half * 256 + i * 8) * 128, smem);
  }
  f32x16 acc0 = {}, acc1 = {};
  float ssum = 0.f, ssq = 0.f;
  const u32 aW = 65536 + kh * 256 * 128 + img128_traddr(lane, nh);
  const u32 aB0 = kh * 256 * 128 + img128_traddr(lane, 0), aB1 = kh * 256 * 128 + img128_traddr(lane, 1);
  const bf16x2 ones = {(__bf16)1.0f, (__bf16)1.0f};
#define KC_CHUNK(c) \
  _Pragma("unroll") for (int s = 4 * (c); s < 4 * (c) + 4; ++s) { \
    bf16x8 wf = tr_frag(smem, aW + s * 2048, aW + s * 2048 + 1024); \
    bf16x8 b0 = tr_frag(smem, aB0 + s * 2048, aB0 + s * 2048 + 1024); \
    bf16x8 b1 = tr_frag(smem, aB1 + s * 2048, aB1 + s * 2048 + 1024); \
    acc0 = mfma32(wf, b0, acc0); acc1 = mfma32(wf, b1, acc1); \
    bf16x8 st = nh ? b1 : b0; \
    _Pragma("unroll") for (int j = 0; j < 4; ++j) { bf16x2 p = {st[2 * j], st[2 * j + 1]}; \
      ssum = __builtin_amdgcn_fdot2_f32_bf16(p, ones, ssum, false); ssq = __builtin_amdgcn_fdot2_f32_bf16(p, p, ssq, false); } \
  }
  WAITV(24); BAR(); KC_CHUNK(0)
  WAITV(16); BAR(); KC_CHUNK(1)
  WAITV(8); BAR(); KC_CHUNK(2)
  WAITV(0); BAR(); KC_CHUNK(3)
  BAR();
  {
    f32x16 snd = kh ? acc0 : acc1;
    char* dst = smem + 65536 + w * 4096 + lane * 16;
#pragma unroll
    for (int q = 0; q < 4; ++q) *(f32x4*)(dst + q * 1024) = (f32x4){snd[4 * q], snd[4 * q + 1], snd[4 * q + 2], snd[4 * q + 3]};
    ssum = swap32_add(ssum); ssq = swap32_add(ssq);
    if (lane < 32) *(f32x2*)(smem + 131072 + ((kh * 2 + nh) * 32 + lane) * 8) = (f32x2){ssum, ssq};
  }
  BAR();
  {
    const int tt = kh;
    f32x16 z = kh ? acc1 : acc0;
    const char* src = smem + 65536 + (w ^ 2) * 4096 + lane * 16;
#pragma unroll
    for (int q = 0; q < 4; ++q) { f32x4 p = *(const f32x4*)(src + q * 1024);
#pragma unroll
      for (int j = 0; j < 4; ++j) z[4 * q + j] += p[j]; }
    f32x2 s0 = *(const f32x2*)(smem + 131072 + ((0 * 2 + tt) * 32 + (lane & 31)) * 8);
    f32x2 s1 = *(const f32x2*)(smem + 131072 + ((1 * 2 + tt) * 32 + (lane & 31)) * 8);
    const float mean = (s0[0] + s1[0]) * (1.f / D);
    const float var = fmaxf((s0[1] + s1[1]) * (1.f / D) - mean * mean, 0.f);
    const float rs1 = __builtin_amdgcn_rsqf(var + 1e-5f);
    const float var2 = var * rs1 * rs1;
    const float rs = rs1 * __builtin_amdgcn_rsqf(var2 + 1e-5f);
    const int T = panel * 2 + tt; const int s_out = ct * 4 + nh * 2;
    const u32 ah = img128_traddr(lane, tt);
#pragma unroll
    for (int sp = 0; sp < 2; ++sp) {
      const int s = s_out + sp;
      bf16x8 hf = tr_frag(smem, ah + s * 2048, ah + s * 2048 + 1024);
      u32x4 hv = __builtin_bit_cast(u32x4, hf);
      u32x4 o, xb;
#pragma unroll
      for (int j = 0; j < 4; ++j) {
        int r = 8 * sp + 2 * j;
        float c0 = cs[r >> 2][r & 3], c1 = cs[(r + 1) >> 2][(r + 1) & 3];
        float g0 = gelu_f((z[r] - mean * c0) * rs), g1 = gelu_f((z[r + 1] - mean * c1) * rs);
        o[j] = pk2(g0, g1);
        xb[j] = pk2((bflo(hv[j]) - mean) * rs1, (bfhi(hv[j]) - mean) * rs1);
      }
      const size_t off = ((size_t)T * 32 + s) * 1024 + lane * 16;
      *(u32x4*)(ws + WS_H1 + 32 * 1024 + off) = o;
      *(u32x4*)(ws + WS_XB + off) = xb;
      *(u32x4*)(ws + WS_HF + off) = hv;
    }
  }
}

constexpr int KD_LDS = 128 * 1024;
__global__ void __launch_bounds__(256) kD(const u16* __restrict__ w2, char* __restrict__ ws) {
  extern __shared__ __attribute__((aligned(16))) char smem[];
  const int tid = threadIdx.x, lane = tid & 63, w = __builtin_amdgcn_readfirstlane(tid >> 6);
  const int b = blockIdx.x; const int panel = (b & 7) * 4 + ((b >> 3) & 3), ct = b >> 5;
  const int kh = w >> 1, nh = w & 1;
  const int T = panel * 2 + kh; const int s_out = ct * 4 + nh * 2;
  u32x4 xb[2];
#pragma unroll
  for (int sp = 0; sp < 2; ++sp) xb[sp] = *(const u32x4*)(ws + WS_XB + ((size_t)T * 32 + s_out + sp) * 1024 + lane * 16);
  {
    const int img = w & 1, half = w >> 1;
    if (img) {
      const char* sl = (const char*)w2 + ct * 128 + (size_t)(half * 256 + (lane >> 3)) * 1024 + img128_srcoff(lane);
#pragma unroll
      for (int i = 0; i < 32; ++i) glds16(sl + (size_t)i * 8 * 1024, 65536 + (half * 256 + i * 8) * 128, smem);
    } else {
      const char* base[2][2];
#pragma unroll
      for (int gg = 0; gg < 2; ++gg)
#pragma unroll
        for (int tt = 0; tt < 2; ++tt) {
          int sh = half ? (gg ? 2 : -2) : (gg ? 1 : -1);
          int tok = panel * 64 + tt * 32 + (lane & 31) + sh + 32;
          base[gg][tt] = ws + WS_H1 + (size_t)(tok >> 5) * 32 * 1024 + ((tok & 31) + 32 * (lane >> 5)) * 16 + (size_t)half * 16 * 1024;
        }
#pragma unroll
      for (int s = 0; s < 16; ++s)
#pragma unroll
        for (int tt = 0; tt < 2; ++tt) glds16(base[s >> 3][tt] + s * 1024, ((half * 16 + s) * 2 + tt) * 1024, smem);
    }
  }
  f32x16 acc0 = {}, acc1 = {};
  const u32 aW = 65536 + kh * 256 * 128 + img128_traddr(lane, nh);
  const u32 aB = kh * 32 * 1024 + lane * 16;
#define KD_CHUNK(c) \
  _Pragma("unroll") for (int s = 4 * (c); s < 4 * (c) + 4; ++s) { \
    bf16x8 wf = tr_frag(smem, aW + s * 2048, aW + s * 2048 + 1024); \
    bf16x8 b0 = *(const bf16x8*)(smem + aB + s * 2048); \
    bf16x8 b1 = *(const bf16x8*)(smem + aB + s * 2048 + 1024); \
    acc0 = mfma32(wf, b0, acc0); acc1 = mfma32(wf, b1, acc1); }
  WAITV(24); BAR(); KD_CHUNK(0)
  WAITV(16); BAR(); KD_CHUNK(1)
  WAITV(8); BAR(); KD_CHUNK(2)
  WAITV(0); BAR(); KD_CHUNK(3)
  BAR();
  {
    f32x16 snd = kh ? acc0 : acc1;
    char* dst = smem + 65536 + w * 4096 + lane * 16;
#pragma unroll
    for (int q = 0; q < 4; ++q) *(f32x4*)(dst + q * 1024) = (f32x4){snd[4 * q], snd[4 * q + 1], snd[4 * q + 2], snd[4 * q + 3]};
  }
  BAR();
  {
    f32x16 z = kh ? acc1 : acc0;
    const char* src = smem + 65536 + (w ^ 2) * 4096 + lane * 16;
#pragma unroll
    for (int q = 0; q < 4; ++q) { f32x4 p = *(const f32x4*)(src + q * 1024);
#pragma unroll
      for (int j = 0; j < 4; ++j) z[4 * q + j] += p[j]; }
    float ssum = 0.f, ssq = 0.f;
#pragma unroll
    for (int sp = 0; sp < 2; ++sp) {
      u32x4 o;
#pragma unroll
      for (int j = 0; j < 4; ++j) {
        int r = 8 * sp + 2 * j;
        u32 p = pk2(bflo(xb[sp][j]) + rbf(z[r]), bfhi(xb[sp][j]) + rbf(z[r + 1]));
        o[j] = p; float a0 = bflo(p), a1 = bfhi(p);
        ssum += a0 + a1; ssq += a0 * a0 + a1 * a1;
      }
      *(u32x4*)(ws + WS_X1 + ((size_t)T * 32 + s_out + sp) * 1024 + lane * 16) = o;
    }
    ssum = swap32_add(ssum); ssq = swap32_add(ssq);
    if (lane < 32) *(f32x2*)(ws + WS_ST + ((size_t)(T * 32 + lane) * 16 + ct * 2 + nh) * 8) = (f32x2){ssum, ssq};
  }
}

constexpr int KE_LDS = 128 * 1024;
__global__ void __launch_bounds__(256) kE(const u16* __restrict__ w3, char* __restrict__ ws) {
  extern __shared__ __attribute__((aligned(16))) char smem[];
  const int tid = threadIdx.x, lane = tid & 63, w = __builtin_amdgcn_readfirstlane(tid >> 6);
  const int b = blockIdx.x; const int rp = (b & 7) * 2 + ((b >> 3) & 1), ct = b >> 4;
  const int tm = w >> 1, tn = w & 1; const int h = lane >> 5;
  f32x4 stv[2][4]; f32x4 cs[2][4];
#pragma unroll
  for (int i = 0; i < 2; ++i) {
    const char* sp = ws + WS_ST + (size_t)((rp * 4 + tm * 2 + i) * 32 + (lane & 31)) * 128 + h * 64;
#pragma unroll
    for (int q = 0; q < 4; ++q) stv[i][q] = *(const f32x4*)(sp + 16 * q);
    const float* c = (const float*)(ws + WS_CS) + D + ct * 128 + (tn * 2 + i) * 32 + 4 * h;
#pragma unroll
    for (int q = 0; q < 4; ++q) cs[i][q] = *(const f32x4*)(c + 8 * q);
  }
  const int img = w & 1, part = w >> 1;
  const char* sl;
  if (img) sl = (const char*)w3 + ct * 256 + (size_t)(part * 32 + (lane >> 4)) * (F * 2) + ((((lane >> 2) & 3) ^ ((lane >> 4) & 3)) * 64) + (lane & 3) * 16;
  else sl = ws + WS_X1 + (size_t)(rp * 4) * 32 * 1024 + (size_t)(part * 2) * 1024 + lane * 16;
  auto stage = [&](int c) {
    const u32 slot = (c & 3) * 32768;
    if (img) {
#pragma unroll
      for (int i = 0; i < 8; ++i) glds16(sl + ((size_t)c * 64 + i * 4) * (F * 2), slot + 16384 + (part * 32 + i * 4) * 256, smem);
    } else {
#pragma unroll
      for (int i = 0; i < 8; ++i) { int s4 = i >> 2, tt = i & 3;
        glds16(sl + ((size_t)tt * 32 + c * 4 + s4) * 1024, slot + ((part * 2 + s4) * 4 + tt) * 1024, smem); }
    }
  };
  f32x16 acc[2][2] = {};
  const int q_ = (lane & 15) >> 2;
  u32 aW[2];
#pragma unroll
  for (int i = 0; i < 2; ++i) aW[i] = 16384 + (4 * h + q_) * 256 + (((tn * 2 + i) ^ q_) * 64) + ((lane >> 4) & 1) * 32 + (lane & 3) * 8;
  const u32 aB = (tm * 2) * 1024 + lane * 16;
  auto compute = [&](int c) {
    const u32 slot = (c & 3) * 32768;
#pragma unroll
    for (int s = 0; s < 4; ++s) {
      bf16x8 w0 = tr_frag(smem, slot + aW[0] + s * 4096, slot + aW[0] + s * 4096 + 2048);
      bf16x8 w1 = tr_frag(smem, slot + aW[1] + s * 4096, slot + aW[1] + s * 4096 + 2048);
      bf16x8 b0 = *(const bf16x8*)(smem + slot + aB + s * 4096);
      bf16x8 b1 = *(const bf16x8*)(smem + slot + aB + s * 4096 + 1024);
      acc[0][0] = mfma32(w0, b0, acc[0][0]); acc[0][1] = mfma32(w0, b1, acc[0][1]);
      acc[1][0] = mfma32(w1, b0, acc[1][0]); acc[1][1] = mfma32(w1, b1, acc[1][1]);
    }
  };
  stage(0); stage(1); stage(2);
#pragma unroll
  for (int c = 0; c < 8; ++c) {
    if (c < 6) WAITV(16); else if (c == 6) WAITV(8); else WAITV(0);
    BAR();
    if (c + 3 < 8) stage(c + 3);
    compute(c);
  }
#pragma unroll
  for (int i = 0; i < 2; ++i) {
    float ssum = 0.f, ssq = 0.f;
#pragma unroll
    for (int q = 0; q < 4; ++q) { ssum += stv[i][q][0] + stv[i][q][2]; ssq += stv[i][q][1] + stv[i][q][3]; }
    ssum = swap32_add(ssum); ssq = swap32_add(ssq);
    const float mean = ssum * (1.f / D);
    const float var = fmaxf(ssq * (1.f / D) - mean * mean, 0.f);
    const float rs = __builtin_amdgcn_rsqf(var + 1e-5f);
    const int T = rp * 4 + tm * 2 + i;
#pragma unroll
    for (int nn = 0; nn < 2; ++nn) {
      const int s_out = ct * 8 + (tn * 2 + nn) * 2;
#pragma unroll
      for (int sp = 0; sp < 2; ++sp) {
        u32x4 o;
#pragma unroll
        for (int j = 0; j < 4; ++j) {
          int r = 8 * sp + 2 * j;
          float c0 = cs[nn][r >> 2][r & 3], c1 = cs[nn][(r + 1) >> 2][(r + 1) & 3];
          o[j] = pk2(gelu_f((acc[nn][i][r] - mean * c0) * rs), gelu_f((acc[nn][i][r + 1] - mean * c1) * rs));
        }
        *(u32x4*)(ws + WS_HH + ((size_t)T * 112 + s_out + sp) * 1024 + lane * 16) = o;
      }
    }
  }
}

constexpr int KF_LDS = 128 * 1024;
__global__ void __launch_bounds__(256) kF(const u16* __restrict__ w4, char* __restrict__ ws, u16* __restrict__ out) {
  extern __shared__ __attribute__((aligned(16))) char smem[];
  const int tid = threadIdx.x, lane = tid & 63, w = __builtin_amdgcn_readfirstlane(tid >> 6);
  const int b = blockIdx.x; const int panel = (b & 7) * 4 + ((b >> 3) & 3), ct = b >> 5;
  const int kh = w >> 1, nh = w & 1; const int h = lane >> 5;
  const int T = panel * 2 + kh; const int s_out = ct * 4 + nh * 2;
  u32x4 x1v[2], hv[2];
#pragma unroll
  for (int sp = 0; sp < 2; ++sp) {
    x1v[sp] = *(const u32x4*)(ws + WS_X1 + ((size_t)T * 32 + s_out + sp) * 1024 + lane * 16);
    hv[sp] = *(const u32x4*)(ws + WS_HF + ((size_t)T * 32 + s_out + sp) * 1024 + lane * 16);
  }
  const int img = w & 1, half = w >> 1;
  const char* sl;
  if (img) sl = (const char*)w4 + ct * 128 + (size_t)(half * 896 + (lane >> 3)) * 1024 + img128_srcoff(lane);
  else sl = ws + WS_HH + ((size_t)(panel * 2) * 112 + half * 56) * 1024 + lane * 16;
  auto stage = [&](int c) {
    const u32 slot = (c & 3) * 32768;
    if (img) {
#pragma unroll
      for (int i = 0; i < 8; ++i) glds16(sl + ((size_t)c * 64 + i * 8) * 1024, slot + 16384 + (half * 64 + i * 8) * 128, smem);
    } else {
#pragma unroll
      for (int i = 0; i < 8; ++i) { int s4 = i >> 1, tt = i & 1;
        glds16(sl + ((size_t)tt * 112 + c * 4 + s4) * 1024, slot + ((half * 4 + s4) * 2 + tt) * 1024, smem); }
    }
  };
  f32x16 acc0 = {}, acc1 = {};
  const u32 aW = 16384 + kh * 64 * 128 + img128_traddr(lane, nh);
  const u32 aB = kh * 8 * 1024 + lane * 16;
  auto compute = [&](int c) {
    const u32 slot = (c & 3) * 32768;
#pragma unroll
    for (int s = 0; s < 4; ++s) {
      bf16x8 wf = tr_frag(smem, slot + aW + s * 2048, slot + aW + s * 2048 + 1024);
      bf16x8 b0 = *(const bf16x8*)(smem + slot + aB + s * 2048);
      bf16x8 b1 = *(const bf16x8*)(smem + slot + aB + s * 2048 + 1024);
      acc0 = mfma32(wf, b0, acc0); acc1 = mfma32(wf, b1, acc1);
    }
  };
  stage(0); stage(1); stage(2);
#pragma unroll 2
  for (int c = 0; c < 12; ++c) {
    WAITV(16); BAR();
    if (c + 3 < 14) stage(c + 3);
    compute(c);
  }
  WAITV(8); BAR(); compute(12);
  WAITV(0); BAR(); compute(13);
  BAR();
  {
    f32x16 snd = kh ? acc0 : acc1;
    char* dst = smem + w * 4096 + lane * 16;
#pragma unroll
    for (int q = 0; q < 4; ++q) *(f32x4*)(dst + q * 1024) = (f32x4){snd[4 * q], snd[4 * q + 1], snd[4 * q + 2], snd[4 * q + 3]};
  }
  BAR();
  {
    f32x16 z = kh ? acc1 : acc0;
    const char* src = smem + (w ^ 2) * 4096 + lane * 16;
#pragma unroll
    for (int q = 0; q < 4; ++q) { f32x4 p = *(const f32x4*)(src + q * 1024);
#pragma unroll
      for (int j = 0; j < 4; ++j) z[4 * q + j] += p[j]; }
    u32x2 o[4];
#pragma unroll
    for (int qq = 0; qq < 4; ++qq) {
      float v[4];
#pragma unroll
      for (int j = 0; j < 4; ++j) {
        int r = 4 * qq + j; int sp = r >> 3, jj = r & 7;
        u32 xp = x1v[sp][jj >> 1], hp = hv[sp][jj >> 1];
        float xv = (jj & 1) ? bfhi(xp) : bflo(xp), hh = (jj & 1) ? bfhi(hp) : bflo(hp);
        v[j] = hh + rbf(xv + rbf(z[r]));
      }
      o[qq] = (u32x2){pk2(v[0], v[1]), pk2(v[2], v[3])};
    }
    char* op = (char*)out + ((size_t)(T * 32 + (lane & 31)) * D + ct * 64 + nh * 32) * 2 + h * 16;
#pragma unroll
    for (int pr = 0; pr < 2; ++pr) {
      u32x2 a = o[2 * pr], c = o[2 * pr + 1];
      auto r0 = __builtin_amdgcn_permlane32_swap(a[0], c[0], false, false);
      auto r1 = __builtin_amdgcn_permlane32_swap(a[1], c[1], false, false);
      *(u32x4*)(op + pr * 32) = (u32x4){r0[0], r1[0], r0[1], r1[1]};
    }
  }
}

extern "C" void kernel_launch(void* const* d_in, const int* in_sizes, int n_in, void* d_out, int out_size, void* d_ws, size_t ws_size,
                              hipStream_t stream) {
  char* ws = (char*)d_ws;
  (void)hipFuncSetAttribute((const void*)kC, hipFuncAttributeMaxDynamicSharedMemorySize, KC_LDS);
  (void)hipFuncSetAttribute((const void*)kD, hipFuncAttributeMaxDynamicSharedMemorySize, KD_LDS);
  (void)hipFuncSetAttribute((const void*)kE, hipFuncAttributeMaxDynamicSharedMemorySize, KE_LDS);
  (void)hipFuncSetAttribute((const void*)kF, hipFuncAttributeMaxDynamicSharedMemorySize, KF_LDS);
  kA<<<KA_GRID, 256, KA_LDS, stream>>>((const u16*)d_in[0], (const u16*)d_in[1], (const u16*)d_in[2], (const u16*)d_in[3], (const u16*)d_in[4],
                                       (const u16*)d_in[5], (const u16*)d_in[8], (const u16*)d_in[10], ws);
  kB<<<256, 256, KB_LDS, stream>>>((const u16*)d_in[6], ws);
  kC<<<256, 256, KC_LDS, stream>>>((const u16*)d_in[8], ws);
  kD<<<256, 256, KD_LDS, stream>>>((const u16*)d_in[9], ws);
  kE<<<224, 256, KE_LDS, stream>>>((const u16*)d_in[10], ws);
  kF<<<256, 256, KF_LDS, stream>>>((const u16*)d_in[11], ws, (u16*)d_out);
}
```

```cpp
#include <hip/hip_runtime.h>
#include <hip/hip_bf16.h>
#include <stdint.h>

typedef unsigned short u16;
typedef unsigned int u32;
typedef unsigned long long u64;
typedef u32 u32x2 __attribute__((ext_vector_type(2)));
typedef u32 u32x4 __attribute__((ext_vector_type(4)));
typedef float f32x2 __attribute__((ext_vector_type(2)));
typedef float f32x4 __attribute__((ext_vector_type(4)));
typedef float f32x16 __attribute__((ext_vector_type(16)));
typedef short s16x4 __attribute__((ext_vector_type(4)));
typedef __bf16 bf16x2 __attribute__((ext_vector_type(2)));
typedef __bf16 bf16x8 __attribute__((ext_vector_type(8)));
#define LAS __attribute__((address_space(3)))
#define GAS __attribute__((address_space(1)))
#define DI __device__ __forceinline__
typedef GAS u64 gu64;
#define RLX_AGENT __ATOMIC_RELAXED, __HIP_MEMORY_SCOPE_AGENT

constexpr int L = 2048, D = 512, NS = 128, F = 1792;
constexpr int KR_LEN = 2112;
constexpr int KR_OFF = 2063;

constexpr u32 WS_UX = 0;
constexpr u32 WS_HP = 4u << 20;
constexpr u32 WS_H1 = 6u << 20;
constexpr u32 WS_X1 = 9u << 20;
constexpr u32 WS_ST = 11u << 20;
constexpr u32 WS_HH = 12u << 20;
constexpr u32 WS_WT1 = 20u << 20;
constexpr u32 WS_WT2 = WS_WT1 + (512u << 10);
constexpr u32 WS_WT4 = 21u << 20;
constexpr u32 WS_WT3 = 23u << 20;
constexpr u32 WS_FL = 25u << 20;
constexpr u32 WS_SEED = WS_FL + 5 * 256 * 128;
constexpr u32 WS_SPAN = 32u << 20;

DI float bf2f(u32 v) { return __uint_as_float(v << 16); }
DI float bflo(u32 v) { return __uint_as_float(v << 16); }
DI float bfhi(u32 v) { return __uint_as_float(v & 0xffff0000u); }
DI u32 pk2(float lo, float hi) { f32x2 v = {lo, hi}; bf16x2 p = __builtin_convertvector(v, bf16x2); return __builtin_bit_cast(u32, p); }
DI u32 pk2v(f32x2 v) { return __builtin_bit_cast(u32, __builtin_convertvector(v, bf16x2)); }
DI float rbf(float f) { return bflo(pk2(f, 0.f) & 0xffffu); }
template <int CTRL, int RMASK>
DI float dppmov(float v) { return __int_as_float(__builtin_amdgcn_update_dpp(0, __float_as_int(v), CTRL, RMASK, 0xf, false)); }
DI float wave_sum(float v) {
  v += dppmov<0x111, 0xf>(v); v += dppmov<0x112, 0xf>(v); v += dppmov<0x114, 0xf>(v); v += dppmov<0x118, 0xf>(v);
  v += dppmov<0x142, 0xa>(v); v += dppmov<0x143, 0xc>(v);
  return __int_as_float(__builtin_amdgcn_readlane(__float_as_int(v), 63));
}
DI float swap32_add(float v) { auto r = __builtin_amdgcn_permlane32_swap(__float_as_uint(v), __float_as_uint(v), false, false); return __uint_as_float(r[0]) + __uint_as_float(r[1]); }
DI float swap16_add(float v) { auto r = __builtin_amdgcn_permlane16_swap(__float_as_uint(v), __float_as_uint(v), false, false); return __uint_as_float(r[0]) + __uint_as_float(r[1]); }
DI f32x2 gelu2(f32x2 z) {
  const float c0 = -2.f * 0.7978845608028654f * 1.4426950408889634f, c1 = c0 * 0.044715f;
  const f32x2 C0 = {c0, c0}, C1 = {c1, c1}, one = {1.f, 1.f};
  f32x2 a = z * __builtin_elementwise_fma(z * z, C1, C0);
  f32x2 e = {__builtin_amdgcn_exp2f(a[0]), __builtin_amdgcn_exp2f(a[1])};
  e = e + one;
  f32x2 r = {__builtin_amdgcn_rcpf(e[0]), __builtin_amdgcn_rcpf(e[1])};
  return z * r;
}
DI u32 gelu_ln2(float a0, float a1, float c0, float c1, float m, float r) {
  const f32x2 M = {-m, -m}, R = {r, r};
  f32x2 z = __builtin_elementwise_fma((f32x2){c0, c1}, M, (f32x2){a0, a1}) * R;
  return pk2v(gelu2(z));
}
DI void glds16(u32 voff, const void* sbase, u32 lds) {
  asm volatile("s_mov_b32 m0, %2\n\ts_nop 0\n\tglobal_load_lds_dwordx4 %0, %1 sc1" :: "v"(voff), "s"(sbase), "s"(lds) : "memory");
}
DI u32 lds_base(char* smem) { return (u32)(uintptr_t)(LAS char*)smem; }
#define WAITV(n) asm volatile("s_waitcnt vmcnt(" #n ")" ::: "memory")
#define WAITV_T(n) asm volatile("s_waitcnt vmcnt(%0)" :: "i"(n) : "memory")
#define SB() __builtin_amdgcn_sched_barrier(0)
#define RAWBAR() do { SB(); __builtin_amdgcn_s_barrier(); SB(); } while (0)
#define BAR() do { SB(); asm volatile("s_waitcnt lgkmcnt(0)" ::: "memory"); __builtin_amdgcn_s_barrier(); asm volatile("" ::: "memory"); SB(); } while (0)
DI bf16x8 tr_frag(char* smem, u32 a0, u32 a1) {
  s16x4 a = __builtin_amdgcn_ds_read_tr16_b64_v4i16((LAS s16x4*)(smem + a0));
  s16x4 b = __builtin_amdgcn_ds_read_tr16_b64_v4i16((LAS s16x4*)(smem + a1));
  return __builtin_bit_cast(bf16x8, __builtin_shufflevector(a, b, 0, 1, 2, 3, 4, 5, 6, 7));
}
DI f32x16 mfma32(bf16x8 a, bf16x8 b, f32x16 c) { return __builtin_amdgcn_mfma_f32_32x32x16_bf16(a, b, c, 0, 0, 0); }
DI f32x4 mfma16(bf16x8 a, bf16x8 b, f32x4 c) { return __builtin_amdgcn_mfma_f32_16x16x32_bf16(a, b, c, 0, 0, 0); }
DI bf16x8 asbf(u32x4 v) { return __builtin_bit_cast(bf16x8, v); }
typedef __amdgpu_buffer_rsrc_t rsrc_t;
DI u32x4 ld16(rsrc_t rs, u32 voff, u32 soff) { return __builtin_amdgcn_raw_buffer_load_b128(rs, voff, soff, 16); }
DI void st16(rsrc_t rs, u32 off, u32x4 v) { __builtin_amdgcn_raw_buffer_store_b128(v, rs, off, 0, 16); }
template <int N, int LO, int HI>
DI void ldw_(u32x4 (&a)[N], rsrc_t rs, u32 base, int lane) {
#pragma unroll
  for (int i = LO; i < HI; ++i) a[i] = ld16(rs, lane * 16, base + i * 1024);
  SB();
}
#define LDW(N, LO, HI, arr, base) ldw_<N, LO, HI>(arr, rs, base, lane)

DI u32 img128_srcoff(int lane) { return ((((lane >> 2) & 1) ^ ((lane >> 4) & 1)) * 64) + (lane & 3) * 16; }
DI u32 img128_traddr(int lane, int ct) {
  int h = lane >> 5, blk = (lane >> 4) & 1, q = (lane & 15) >> 2, p = lane & 3;
  return (4 * h + q) * 128 + ((ct ^ ((q >> 1) & 1)) * 64) + blk * 32 + p * 8;
}

struct KTab { u16 v[KR_LEN + 64]; };
constexpr int bf16_round_pos(int t) {
  if (t < 256) return t;
  int sh = t < 512 ? 1 : t < 1024 ? 2 : 3;
  int q = t >> sh, r = t & ((1 << sh) - 1), half = 1 << (sh - 1);
  if (r > half || (r == half && (q & 1))) ++q;
  return q << sh;
}
constexpr KTab make_ktab() {
  KTab t{};
  for (int i = 0; i < KR_LEN + 64; ++i) {
    int tau = KR_OFF - i; int idx = 1024;
    if (tau >= 0 && tau < L) {
      int p = bf16_round_pos(tau); int m = 0, col = 0;
      if (p < 256) { m = p & 7; col = p >> 3; }
      else if (p < 512) { m = 8 + ((p & 7) >> 1); col = (p - 256) >> 3; }
      else if (p < 1024) { m = 12 + 2 * ((p - 512) >> 8) + ((p >> 2) & 1); col = (p & 255) >> 3; }
      else { m = 16 + ((p - 1024) >> 8); col = (p & 255) >> 3; }
      idx = m * 32 + col;
    }
    t.v[i] = (u16)idx;
  }
  return t;
}
__device__ const KTab g_ktab = make_ktab();

struct cpx { float r, i; };
DI cpx cmul(cpx a, cpx b) { return {a.r * b.r - a.i * b.i, a.r * b.i + a.i * b.r}; }
DI cpx cexp_p(float ar, float ai_rev, float p) {
  float e = __builtin_amdgcn_exp2f(ar * p * 1.4426950408889634f);
  float rv = ai_rev * p; rv -= floorf(rv);
  return {e * __builtin_amdgcn_cosf(rv), e * __builtin_amdgcn_sinf(rv)};
}

constexpr int KC_STR = 4416, KC_CH = 8 * KC_STR;
constexpr int U_QS = 1568, U_CH = 4 * U_QS;
constexpr int LD_U = 2 * KC_CH;
constexpr int LD_X = LD_U + 2 * U_CH;
constexpr int LD_KV = 96 * 1024;
constexpr int LD_KS = LD_KV + 16384;
constexpr int LD_R = LD_KS + 2 * 4224;
constexpr int LD_LN = 132 * 1024;
constexpr int LD_EX = 128 * 1024;
constexpr int LD_SM = LD_EX + 16384;
constexpr int LD_CS1 = LD_SM + 1024, LD_CS3 = LD_CS1 + 1024;
constexpr int LDS_BYTES = 148 * 1024 + 1024;
static_assert(LD_R + 2 * (KR_LEN + 64) * 2 <= LD_LN, "lds");
static_assert(LD_LN + 16384 <= LDS_BYTES && LD_CS3 + 1024 <= LDS_BYTES, "lds");

constexpr unsigned SPIN_CAP = 1u << 18;
constexpr u64 TAG_INC = 0x9E3779B97F4A7C15ull;

DI void publish(char* ws, int phase, int id, u64 tag) {
  RAWBAR();
  if (threadIdx.x == 0) __hip_atomic_store((gu64*)(ws + WS_FL + (phase * 256 + id) * 128), tag, RLX_AGENT);
}
DI void wait_flags(char* ws, int phase, int idx, int w, u64 tag) {
  if (w == 0) {
    for (unsigned spins = 0; spins < SPIN_CAP; ++spins) {
      u64 f = tag;
      if (idx >= 0) f = __hip_atomic_load((gu64*)(ws + WS_FL + (phase * 256 + idx) * 128), RLX_AGENT);
      if (__all(f == tag)) break;
    }
  }
  RAWBAR();
}
DI void wait_all(char* ws, int phase, int w, int lane, u64 tag) {
  if (w == 0) {
    gu64* fp = (gu64*)(ws + WS_FL + (phase * 256 + lane) * 128);
    for (unsigned spins = 0; spins < SPIN_CAP; ++spins) {
      u64 f0 = __hip_atomic_load(fp, RLX_AGENT), f1 = __hip_atomic_load(fp + 64 * 16, RLX_AGENT);
      u64 f2 = __hip_atomic_load(fp + 128 * 16, RLX_AGENT), f3 = __hip_atomic_load(fp + 192 * 16, RLX_AGENT);
      if (__all(f0 == tag && f1 == tag && f2 == tag && f3 == tag)) break;
    }
  }
  RAWBAR();
}

#define NW3A 8
#define NW4A 46

__global__ void __launch_bounds__(256, 1) kmain(const u16* __restrict__ x, const u16* __restrict__ logA, const u16* __restrict__ Aim,
                                                const u16* __restrict__ Cr, const u16* __restrict__ Ci, const u16* __restrict__ logdt,
                                                const u16* __restrict__ dskip, const u16* __restrict__ w1g, const u16* __restrict__ w2g,
                                                const u16* __restrict__ w3g, const u16* __restrict__ w4g, char* ws, u16* out) {
  extern __shared__ __attribute__((aligned(16))) char smem[];
  const int tid = threadIdx.x, lane = tid & 63, w = __builtin_amdgcn_readfirstlane(tid >> 6);
  const int b = blockIdx.x, xcd = b & 7, idx = b >> 3;
  const u32 lb = lds_base(smem);
  const rsrc_t rs = __builtin_amdgcn_make_buffer_rsrc(ws, 0, WS_SPAN, 0x00020000);
  const int h = lane >> 5;
  u64 tag;
  { u64 sd = __hip_atomic_load((gu64*)(ws + WS_SEED), RLX_AGENT);
    u32 lo = __builtin_amdgcn_readfirstlane((u32)sd), hi = __builtin_amdgcn_readfirstlane((u32)(sd >> 32));
    tag = (((u64)hi << 32) | lo) + TAG_INC; }

  const int panel = xcd * 4 + (idx & 3), ct = idx >> 2, me = panel * 8 + ct;
  const int kh = w >> 1, nh = w & 1, tto = kh ^ nh;
  const u32 w1p = WS_WT1 + ((ct * 2 + nh) * 2 + kh) * 16384, w2p = WS_WT2 + ((ct * 2 + nh) * 2 + kh) * 16384;
  const u32 w3p = WS_WT3 + (ct * 4 + w) * 65536, w4p = WS_WT4 + ((ct * 2 + nh) * 2 + kh) * 56 * 1024;
  u32x4 w1[16], w2[16], w3[64];

  const int c0 = 2 * (xcd * 32 + idx);
  const int chl = w >> 1, ih = w & 1, ch = c0 + chl;
  {
    u32x4 xv[2];
#pragma unroll
    for (int r = 0; r < 2; ++r) xv[r] = *(const GAS u32x4*)(x + (size_t)(8 * b + 2 * w + r) * D + lane * 8);
    const int n = 64 * ih + lane;
    const u32 p_logA = logA[ch * NS + n], p_Aim = Aim[ch * NS + n], p_Cr = Cr[ch * NS + n], p_Ci = Ci[ch * NS + n], p_dt = logdt[ch];
    u32x4 ktab[3];
#pragma unroll
    for (int it = 0; it < 3; ++it) { int unit = it * 256 + tid; int u8 = unit >= KR_LEN / 8 ? unit - KR_LEN / 8 : unit; if (u8 >= KR_LEN / 8) u8 = 0; ktab[it] = *(const GAS u32x4*)(g_ktab.v + u8 * 8); }
    int u = w * 256 + b; const bool has_unit = u < 576;
    const u16* W = w1g; int N = D, ku = 0, nu = 0, kind = 0;
    if (u < 64) { W = w1g; ku = u >> 3; nu = u & 7; kind = 0; }
    else if (u < 128) { u -= 64; W = w2g; ku = u >> 3; nu = u & 7; kind = 1; }
    else if (u < 352) { u -= 128; W = w4g; ku = u >> 3; nu = u & 7; kind = 2; }
    else if (u < 576) { u -= 352; W = w3g; N = F; ku = u / 28; nu = u % 28; kind = 3; }
    u32x4 wv[8];
    if (has_unit) {
      const u16* src = W + (size_t)(ku * 64 + (lane >> 3)) * N + nu * 64 + (lane & 7) * 8;
#pragma unroll
      for (int i = 0; i < 8; ++i) wv[i] = *(const GAS u32x4*)(src + (size_t)i * 8 * N);
    }
    SB();
    {
      float uo[2][8];
#pragma unroll
      for (int r = 0; r < 2; ++r) {
        float v[8];
#pragma unroll
        for (int j = 0; j < 4; ++j) { v[2 * j] = bflo(xv[r][j]); v[2 * j + 1] = bfhi(xv[r][j]); }
        float s = 0;
#pragma unroll
        for (int j = 0; j < 8; ++j) s += v[j];
        float mean = wave_sum(s) * (1.f / D);
        float q = 0;
#pragma unroll
        for (int j = 0; j < 8; ++j) { float d = v[j] - mean; q += d * d; }
        float var = wave_sum(q) * (1.f / D);
        float mb = rbf(mean), sb = rbf(sqrtf(rbf(rbf(var) + 1e-5f)));
        float rsb = 1.f / sb;
#pragma unroll
        for (int j = 0; j < 8; ++j) uo[r][j] = rbf(v[j] - mb) * rsb;
      }
      const int sw = lane & 7;
#pragma unroll
      for (int j = 0; j < 8; ++j) {
        char* row = smem + LD_LN + (lane * 8 + j) * 32;
        const u32 x0 = xv[0][j >> 1], x1 = xv[1][j >> 1];
        *(u32*)(row + ((w ^ sw) * 4)) = pk2(uo[0][j], uo[1][j]);
        *(u32*)(row + (((4 + w) ^ sw) * 4)) = (j & 1) ? ((x0 >> 16) | (x1 & 0xffff0000u)) : ((x0 & 0xffffu) | (x1 << 16));
      }
    }
    BAR();
#pragma unroll
    for (int it = 0; it < 4; ++it) {
      const int unit = it * 256 + tid, cc = unit >> 1, half = unit & 1, s = (cc >> 3) & 7;
      u32x4 v = *(const u32x4*)(smem + LD_LN + cc * 32 + (((4 * half) ^ (s & 4)) * 4));
      if (s & 1) v = (u32x4){v[1], v[0], v[3], v[2]};
      if (s & 2) v = (u32x4){v[2], v[3], v[0], v[1]};
      st16(rs, WS_UX + b * 16384 + unit * 16, v);
    }
    if (has_unit) {
      char* img = smem + LD_KV + w * 8192; const u32 imgo = LD_KV + w * 8192;
      const int pc = lane & 7;
#pragma unroll
      for (int i = 0; i < 8; ++i) {
        const int r = i * 8 + (lane >> 3);
        const int pp = (kind == 3) ? ((((pc >> 1) ^ ((r >> 1) & 3)) << 1) | (pc & 1)) : ((((pc >> 2) ^ ((r >> 1) & 1)) << 2) | (pc & 3));
        *(u32x4*)(img + r * 128 + pp * 16) = wv[i];
      }
      if (kind != 3) {
        const int ksh = kind == 2 ? 56 : 16;
        const u32 dst = kind == 0 ? WS_WT1 : kind == 1 ? WS_WT2 : WS_WT4;
#pragma unroll
        for (int hn = 0; hn < 2; ++hn) {
          const u32 a = imgo + img128_traddr(lane, hn);
#pragma unroll
          for (int s4 = 0; s4 < 4; ++s4) {
            bf16x8 f = tr_frag(smem, a + s4 * 2048, a + s4 * 2048 + 1024);
            const int s = ku * 4 + s4; const int k2 = s >= ksh; const int sl = s - k2 * ksh;
            st16(rs, dst + (((nu * 2 + hn) * 2 + k2) * ksh + sl) * 1024 + lane * 16, __builtin_bit_cast(u32x4, f));
          }
        }
      } else {
        const int g = lane >> 4, q = (lane & 15) >> 2, p = lane & 3;
#pragma unroll
        for (int c16 = 0; c16 < 4; ++c16) {
          const int j = nu * 4 + c16; const int tc = j / 14, r14 = j % 14;
#pragma unroll
          for (int sp = 0; sp < 2; ++sp) {
            const int r0 = 32 * sp + 16 * (g >> 1) + 4 * (g & 1) + q, r1 = r0 + 8;
            bf16x8 f = tr_frag(smem, imgo + r0 * 128 + ((c16 ^ ((r0 >> 1) & 3)) * 32) + p * 8, imgo + r1 * 128 + ((c16 ^ ((r1 >> 1) & 3)) * 32) + p * 8);
            const u32 dst = WS_WT3 + tc * 4 * 65536 + (ku * 2 + sp) * 1024 + lane * 16;
            if (r14 < 12) st16(rs, dst + (r14 / 3) * 65536 + (r14 % 3) * 16384, __builtin_bit_cast(u32x4, f));
            else { const int w0 = 2 * (r14 - 12); st16(rs, dst + w0 * 65536 + 3 * 16384, __builtin_bit_cast(u32x4, f)); st16(rs, dst + (w0 + 1) * 65536 + 3 * 16384, __builtin_bit_cast(u32x4, f)); }
          }
        }
      }
    }
    if (b >= 252) {
      const u32 dst = WS_H1 + ((b & 2) ? 65u * 32 * 1024 : 0u) + (b & 1) * 16384 + tid * 16; const u32x4 z = {0, 0, 0, 0};
#pragma unroll
      for (int i = 0; i < 4; ++i) st16(rs, dst + i * 4096, z);
    }
    WAITV(0);
    publish(ws, 0, b, tag);
    const float dt = rbf(expf(bf2f(p_dt)));
    const float ar = -rbf(expf(bf2f(p_logA))) * dt;
    const float ai = bf2f(p_Aim) * dt * 0.15915494309189535f;
    const cpx c = {2.f * bf2f(p_Cr) * dt, 2.f * bf2f(p_Ci) * dt};
    char* tb = smem + w * (53 * 272);
    char* rowB = tb + lane * 4;
    char* rowA = tb + 32 * 272 + lane * 4;
    cpx w1c = cexp_p(ar, ai, 1.f), w8 = cexp_p(ar, ai, 8.f), e256 = cexp_p(ar, ai, 256.f);
    {
      cpx e0 = {1.f, 0.f}, e1 = cexp_p(ar, ai, 128.f);
#pragma unroll
      for (int col = 0; col < 16; ++col) {
        *(u32*)(rowB + col * 272) = pk2(e0.r, e0.i); *(u32*)(rowB + (16 + col) * 272) = pk2(e1.r, e1.i);
        e0 = cmul(e0, w8); e1 = cmul(e1, w8);
      }
      cpx e = c;
#pragma unroll
      for (int m = 0; m < 8; ++m) { *(u32*)(rowA + m * 272) = pk2(e.r, -e.i); e = cmul(e, w1c); }
      cpx w2c = cmul(w1c, w1c), w4c = cmul(w2c, w2c);
      cpx c256 = cmul(c, e256); e = c256;
#pragma unroll
      for (int m = 8; m < 12; ++m) { *(u32*)(rowA + m * 272) = pk2(e.r, -e.i); e = cmul(e, w2c); }
      cpx c512 = cmul(c256, e256), c768 = cmul(c512, e256);
      *(u32*)(rowA + 12 * 272) = pk2(c512.r, -c512.i); e = cmul(c512, w4c); *(u32*)(rowA + 13 * 272) = pk2(e.r, -e.i);
      *(u32*)(rowA + 14 * 272) = pk2(c768.r, -c768.i); e = cmul(c768, w4c); *(u32*)(rowA + 15 * 272) = pk2(e.r, -e.i);
      e = cmul(c768, e256);
#pragma unroll
      for (int m = 16; m < 21; ++m) { *(u32*)(rowA + m * 272) = pk2(e.r, -e.i); e = cmul(e, e256); }
    }
    f32x16 kacc = {};
    {
      const int m = lane & 31;
#pragma unroll
      for (int s = 0; s < 8; ++s) {
        bf16x8 af = *(const bf16x8*)(smem + w * (53 * 272) + (32 + m) * 272 + 32 * s + 16 * h);
        bf16x8 bf = *(const bf16x8*)(smem + w * (53 * 272) + m * 272 + 32 * s + 16 * h);
        kacc = mfma32(af, bf, kacc);
      }
    }
    {
      float* kv = (float*)(smem + LD_KV + w * 4096);
      const int col = lane & 31;
#pragma unroll
      for (int r = 0; r < 16; ++r) kv[((r & 3) + 8 * (r >> 2) + 4 * h) * 32 + col] = kacc[r];
    }
    BAR();
    {
#pragma unroll
      for (int it = 0; it < 2; ++it) {
        const int i4 = it * 256 + tid, c2 = i4 >> 8, e4 = i4 & 255;
        f32x4 a = *(const f32x4*)(smem + LD_KV + (2 * c2) * 4096 + e4 * 16), bq = *(const f32x4*)(smem + LD_KV + (2 * c2 + 1) * 4096 + e4 * 16);
        *(f32x4*)(smem + LD_KS + c2 * 4224 + e4 * 16) = a + bq;
      }
      if (tid < 2) *(float*)(smem + LD_KS + tid * 4224 + 4096) = 0.f;
    }
    BAR();
#pragma unroll
    for (int it = 0; it < 3; ++it) {
      const int unit = it * 256 + tid;
      if (unit < 2 * (KR_LEN / 8)) {
        const int c2 = unit >= KR_LEN / 8, u8 = unit - c2 * (KR_LEN / 8);
        const float* kv = (const float*)(smem + LD_KS + c2 * 4224);
        u32x4 o;
#pragma unroll
        for (int j = 0; j < 4; ++j) o[j] = pk2(kv[ktab[it][j] & 0xffffu], kv[ktab[it][j] >> 16]);
        *(u32x4*)(smem + LD_R + c2 * (KR_LEN + 64) * 2 + u8 * 16) = o;
      }
    }
    { const int c2 = tid >> 7, q = (tid >> 5) & 3, cc = tid & 31; *(u32x4*)(smem + LD_U + c2 * U_CH + q * U_QS + cc * 16) = (u32x4){0, 0, 0, 0}; }
    BAR();
#pragma unroll
    for (int it = 0; it < 3; ++it) {
      const int unit = it * 256 + tid;
      if (unit < 2 * (KR_LEN / 8)) {
        const int c2 = unit >= KR_LEN / 8, u8 = unit - c2 * (KR_LEN / 8);
        const char* src = smem + LD_R + c2 * (KR_LEN + 64) * 2 + u8 * 16;
        const u32x4 k0 = *(const u32x4*)src, k1 = *(const u32x4*)(src + 16);
        const u32 d[8] = {k0[0], k0[1], k0[2], k0[3], k1[0], k1[1], k1[2], k1[3]};
        u32 a[7];
#pragma unroll
        for (int k = 0; k < 7; ++k) a[k] = __builtin_amdgcn_alignbyte(d[k + 1], d[k], 2);
        char* dst = smem + c2 * KC_CH + u8 * 16;
#pragma unroll
        for (int k = 0; k < 4; ++k) {
          *(u32x4*)(dst + (7 - 2 * k) * KC_STR) = (u32x4){d[k], d[k + 1], d[k + 2], d[k + 3]};
          *(u32x4*)(dst + (6 - 2 * k) * KC_STR) = (u32x4){a[k], a[k + 1], a[k + 2], a[k + 3]};
        }
      }
    }
  }
  wait_all(ws, 0, w, lane, tag);
  if (b == 0 && tid == 0) __hip_atomic_store((gu64*)(ws + WS_SEED), tag, RLX_AGENT);
  {
    const float dsk = bf2f(dskip[ch]);
    {
      u32x4 gv[4];
#pragma unroll
      for (int i = 0; i < 4; ++i) gv[i] = ld16(rs, (64 * w + 16 * i + (lane >> 2)) * 16384 + (lane & 3) * 16, WS_UX + c0 * 32);
#pragma unroll
      for (int i = 0; i < 4; ++i) {
        const int tb = 64 * w + 16 * i + (lane >> 2), pc = lane & 3;
        *(u32x4*)(smem + ((pc & 1) ? LD_X : LD_U) + (pc >> 1) * U_CH + (tb & 3) * U_QS + (32 + (tb >> 2)) * 16) = gv[i];
      }
    }
    BAR();
    const int n = lane & 31;
    f32x16 acc0 = {}, acc1 = {};
    {
      const u32 abase = chl * KC_CH + (n & 7) * KC_STR + 16 * (257 + h - (n >> 3)) - 32 * (32 * ih - 1) - 32 * 95;
      const u32 ubase = LD_U + chl * U_CH + h * U_QS + (32 + n - 16 * ih) * 16 - 16 * 16;
      bf16x8 A1[3], A2[3], b0[3], b1[3];
      auto ld = [&](int ii) {
        const int k = ii % 3;
        A1[k] = *(const bf16x8*)(smem + abase + 32 * (95 - ii));
        A2[k] = *(const bf16x8*)(smem + abase + 32 * (95 - 64 - ii));
        const u32 pb = (ii & 1) ? (ubase + (16 - (ii - 1) / 2) * 16) : (ubase + 2 * U_QS + (16 - ii / 2) * 16);
        b0[k] = *(const bf16x8*)(smem + pb); b1[k] = *(const bf16x8*)(smem + pb + 32 * 16);
      };
      ld(0); ld(1);
#define CONV_STEP(ii) { if ((ii) + 2 < 32) ld((ii) + 2); SB(); \
        acc0 = mfma32(A1[(ii) % 3], b0[(ii) % 3], acc0); acc1 = mfma32(A1[(ii) % 3], b1[(ii) % 3], acc1); acc1 = mfma32(A2[(ii) % 3], b0[(ii) % 3], acc1); SB(); \
        if ((ii) < 16) LDW(16, (ii) < 16 ? (ii) : 0, (ii) < 16 ? (ii) + 1 : 0, w1, w1p); else LDW(16, (ii) >= 16 ? (ii) - 16 : 0, (ii) >= 16 ? (ii) - 15 : 0, w2, w2p); }
      CONV_STEP(0) CONV_STEP(1) CONV_STEP(2) CONV_STEP(3) CONV_STEP(4) CONV_STEP(5) CONV_STEP(6) CONV_STEP(7)
      CONV_STEP(8) CONV_STEP(9) CONV_STEP(10) CONV_STEP(11) CONV_STEP(12) CONV_STEP(13) CONV_STEP(14) CONV_STEP(15)
      CONV_STEP(16) CONV_STEP(17) CONV_STEP(18) CONV_STEP(19) CONV_STEP(20) CONV_STEP(21) CONV_STEP(22) CONV_STEP(23)
      CONV_STEP(24) CONV_STEP(25) CONV_STEP(26) CONV_STEP(27) CONV_STEP(28) CONV_STEP(29) CONV_STEP(30) CONV_STEP(31)
    }
    BAR();
    {
      f32x16 snd = ih ? acc0 : acc1;
      char* dst = smem + w * 4096 + lane * 16;
#pragma unroll
      for (int q = 0; q < 4; ++q) *(f32x4*)(dst + q * 1024) = (f32x4){snd[4 * q], snd[4 * q + 1], snd[4 * q + 2], snd[4 * q + 3]};
    }
    BAR();
    {
      f32x16 y = ih ? acc1 : acc0;
      const char* src = smem + (w ^ 1) * 4096 + lane * 16;
      u32x2 o[4];
#pragma unroll
      for (int q = 0; q < 4; ++q) {
        const f32x4 p = *(const f32x4*)(src + q * 1024);
        const u32 io = chl * U_CH + q * U_QS + (32 + 32 * ih + n) * 16 + 8 * h;
        const u32x2 uu = *(const u32x2*)(smem + LD_U + io), xx = *(const u32x2*)(smem + LD_X + io);
#pragma unroll
        for (int e = 0; e < 2; ++e) {
          const float y0 = rbf(y[4 * q + 2 * e] + p[2 * e]), y1 = rbf(y[4 * q + 2 * e + 1] + p[2 * e + 1]);
          const float t0 = rbf(y0 + rbf(bflo(uu[e]) * dsk)), t1 = rbf(y1 + rbf(bfhi(uu[e]) * dsk));
          o[q][e] = pk2(bflo(xx[e]) + t0, bfhi(xx[e]) + t1);
        }
      }
      const u32 hp = WS_HP + (16 * ih + (n >> 1)) * 65536 + ch * 128 + (32 * (n & 1) + 8 * h) * 2;
#pragma unroll
      for (int pr = 0; pr < 2; ++pr) {
        auto r0 = __builtin_amdgcn_permlane32_swap(o[2 * pr][0], o[2 * pr + 1][0], false, false);
        auto r1 = __builtin_amdgcn_permlane32_swap(o[2 * pr][1], o[2 * pr + 1][1], false, false);
        st16(rs, hp + pr * 32, (u32x4){r0[0], r1[0], r0[1], r1[1]});
      }
    }
    SB();
    LDW(64, 0, NW3A, w3, w3p);
    WAITV_T(NW3A);
    publish(ws, 1, b, tag);
    if (w != 0) LDW(64, NW3A, 40, w3, w3p); else LDW(64, NW3A, 20, w3, w3p);
    {
      const bf16x8 onesv = {(__bf16)1.f, (__bf16)1.f, (__bf16)1.f, (__bf16)1.f, (__bf16)1.f, (__bf16)1.f, (__bf16)1.f, (__bf16)1.f};
      f32x16 cacc = {};
#pragma unroll
      for (int s = 0; s < 16; ++s) cacc = mfma32(asbf(w1[s]), onesv, cacc);
      if ((lane & 31) == 0) {
        float* dst = (float*)(smem + LD_CS1) + (kh * 2 + nh) * 32 + 4 * h;
#pragma unroll
        for (int q = 0; q < 4; ++q) *(f32x4*)(dst + 8 * q) = (f32x4){cacc[4 * q], cacc[4 * q + 1], cacc[4 * q + 2], cacc[4 * q + 3]};
      }
    }
    wait_all(ws, 1, w, lane, tag);
  }

  const int T = panel * 2 + tto; const int s_out = ct * 4 + nh * 2;
  char* exw = smem + LD_EX + w * 4096 + lane * 16;
  const char* exr = smem + LD_EX + (w ^ 2) * 4096 + lane * 16;
  u32x4 hres[2], xb[2], x1r[2];
  {
    {
      const int half = w >> 1, sub = w & 1;
      const char* sl = ws + WS_HP + (size_t)panel * 65536 + (half * 256 + sub * 32) * 128;
      const u32 vo = (lane >> 3) * 128 + img128_srcoff(lane);
#pragma unroll
      for (int c = 0; c < 3; ++c)
#pragma unroll
        for (int i = 0; i < 4; ++i) glds16(vo, sl + (c * 64 + i * 8) * 128, lb + (half * 256 + sub * 32 + c * 64 + i * 8) * 128);
      SB();
      f32x16 acc0 = {}, acc1 = {};
      float sa0 = 0.f, sa1 = 0.f, sq0 = 0.f, sq1 = 0.f;
      const u32 aB0 = kh * 256 * 128 + img128_traddr(lane, tto), aB1 = kh * 256 * 128 + img128_traddr(lane, tto ^ 1);
      const bf16x2 ones = {(__bf16)1.0f, (__bf16)1.0f};
      bf16x8 b0[2], b1[2];
      WAITV(8); RAWBAR();
      b0[0] = tr_frag(smem, aB0, aB0 + 1024); b1[0] = tr_frag(smem, aB1, aB1 + 1024);
#pragma unroll
      for (int s = 0; s < 16; ++s) {
        if (s + 1 < 16) {
          if (s + 1 == 4) { WAITV(7); RAWBAR(); } else if (s + 1 == 8) { WAITV(4); RAWBAR(); } else if (s + 1 == 12) { WAITV(0); RAWBAR(); }
          b0[(s + 1) & 1] = tr_frag(smem, aB0 + (s + 1) * 2048, aB0 + (s + 1) * 2048 + 1024);
          b1[(s + 1) & 1] = tr_frag(smem, aB1 + (s + 1) * 2048, aB1 + (s + 1) * 2048 + 1024);
        }
        SB();
        acc0 = mfma32(asbf(w1[s]), b0[s & 1], acc0); acc1 = mfma32(asbf(w1[s]), b1[s & 1], acc1);
        SB();
        if (s < 4) glds16(vo, sl + (3 * 64 + s * 8) * 128, lb + (half * 256 + sub * 32 + 3 * 64 + s * 8) * 128);
        { const bf16x8 st = b0[s & 1];
          const bf16x2 p0 = {st[0], st[1]}, p1 = {st[2], st[3]}, p2 = {st[4], st[5]}, p3 = {st[6], st[7]};
          sa0 = __builtin_amdgcn_fdot2_f32_bf16(p0, ones, sa0, false); sq0 = __builtin_amdgcn_fdot2_f32_bf16(p0, p0, sq0, false);
          sa1 = __builtin_amdgcn_fdot2_f32_bf16(p1, ones, sa1, false); sq1 = __builtin_amdgcn_fdot2_f32_bf16(p1, p1, sq1, false);
          sa0 = __builtin_amdgcn_fdot2_f32_bf16(p2, ones, sa0, false); sq0 = __builtin_amdgcn_fdot2_f32_bf16(p2, p2, sq0, false);
          sa1 = __builtin_amdgcn_fdot2_f32_bf16(p3, ones, sa1, false); sq1 = __builtin_amdgcn_fdot2_f32_bf16(p3, p3, sq1, false); }
        if (s >= 12 && w == 0) { if (s == 12) LDW(64, 20, 25, w3, w3p); else if (s == 13) LDW(64, 25, 30, w3, w3p); else if (s == 14) LDW(64, 30, 35, w3, w3p); else LDW(64, 35, 40, w3, w3p); }
        SB();
      }
      float ssum = sa0 + sa1, ssq = sq0 + sq1;
#pragma unroll
      for (int q = 0; q < 4; ++q) *(f32x4*)(exw + q * 1024) = (f32x4){acc1[4 * q], acc1[4 * q + 1], acc1[4 * q + 2], acc1[4 * q + 3]};
      ssum = swap32_add(ssum); ssq = swap32_add(ssq);
      if (lane < 32) *(f32x2*)(smem + LD_SM + ((kh * 2 + tto) * 32 + lane) * 8) = (f32x2){ssum, ssq};
      BAR();
      f32x16 z = acc0;
#pragma unroll
      for (int q = 0; q < 4; ++q) { f32x4 p = *(const f32x4*)(exr + q * 1024);
#pragma unroll
        for (int j = 0; j < 4; ++j) z[4 * q + j] += p[j]; }
      f32x4 cs[4];
#pragma unroll
      for (int q = 0; q < 4; ++q) cs[q] = *(const f32x4*)(smem + LD_CS1 + ((0 * 2 + nh) * 32 + 8 * q + 4 * h) * 4) + *(const f32x4*)(smem + LD_CS1 + ((1 * 2 + nh) * 32 + 8 * q + 4 * h) * 4);
      f32x2 s0 = *(const f32x2*)(smem + LD_SM + ((0 * 2 + tto) * 32 + (lane & 31)) * 8);
      f32x2 s1 = *(const f32x2*)(smem + LD_SM + ((1 * 2 + tto) * 32 + (lane & 31)) * 8);
      const float mean = (s0[0] + s1[0]) * (1.f / D);
      const float var = fmaxf((s0[1] + s1[1]) * (1.f / D) - mean * mean, 0.f);
      const float rs1 = __builtin_amdgcn_rsqf(var + 1e-5f);
      const float var2 = var * rs1 * rs1;
      const float rsc = rs1 * __builtin_amdgcn_rsqf(var2 + 1e-5f);
      const u32 ah = img128_traddr(lane, tto);
      const f32x2 R1 = {rs1, rs1}, MR = {-mean * rs1, -mean * rs1};
#pragma unroll
      for (int sp = 0; sp < 2; ++sp) {
        const int s = s_out + sp;
        bf16x8 hf = tr_frag(smem, ah + s * 2048, ah + s * 2048 + 1024);
        u32x4 hv = __builtin_bit_cast(u32x4, hf);
        u32x4 o;
#pragma unroll
        for (int j = 0; j < 4; ++j) {
          int r = 8 * sp + 2 * j;
          o[j] = gelu_ln2(z[r], z[r + 1], cs[r >> 2][r & 3], cs[(r + 1) >> 2][(r + 1) & 3], mean, rsc);
          xb[sp][j] = pk2v(__builtin_elementwise_fma((f32x2){bflo(hv[j]), bfhi(hv[j])}, R1, MR));
        }
        hres[sp] = hv;
        st16(rs, WS_H1 + 32 * 1024 + (T * 32 + s) * 1024 + lane * 16, o);
      }
    }
    SB();
    LDW(64, 40, 46, w3, w3p);
    WAITV(6);
    publish(ws, 2, me, tag);
    if (w != 0) LDW(64, 46, 64, w3, w3p); else LDW(64, 46, 52, w3, w3p);
  }
  {
    {
      int ix = -1;
      if (lane < 8) ix = panel * 8 + lane;
      else if (lane < 12) { if (panel > 0) ix = (panel - 1) * 8 + ((lane & 1) | ((lane & 2) << 1)); }
      else if (lane < 16) { if (panel < 31) ix = (panel + 1) * 8 + 2 + ((lane & 1) | ((lane & 2) << 1)); }
      wait_flags(ws, 2, ix, w, tag);
    }
    f32x16 c0a = {}, c1a = {};
    {
      const int half = w >> 1, tt = w & 1;
      u32 vo[2];
#pragma unroll
      for (int gg = 0; gg < 2; ++gg) {
        int sh = half ? (gg ? 2 : -2) : (gg ? 1 : -1);
        int tok = tt * 32 + (lane & 31) + sh + 32;
        vo[gg] = (tok >> 5) * 32 * 1024 + ((tok & 31) + 32 * (lane >> 5)) * 16;
      }
      const char* sl = ws + WS_H1 + (size_t)(panel * 2) * 32 * 1024 + (size_t)half * 16 * 1024;
#pragma unroll
      for (int s = 0; s < 12; ++s) glds16(vo[s >> 3], sl + s * 1024, lb + ((half * 16 + s) * 2 + tt) * 1024);
      SB();
      const u32 aB = kh * 32 * 1024 + lane * 16 + tto * 1024, aBo = aB ^ 1024;
      bf16x8 p0[2], p1[2];
      WAITV(8); RAWBAR();
      p0[0] = *(const bf16x8*)(smem + aB); p1[0] = *(const bf16x8*)(smem + aBo);
#pragma unroll
      for (int s = 0; s < 16; ++s) {
        if (s + 1 < 16) {
          if (s + 1 == 4) { WAITV(7); RAWBAR(); } else if (s + 1 == 8) { WAITV(4); RAWBAR(); } else if (s + 1 == 12) { WAITV(0); RAWBAR(); }
          p0[(s + 1) & 1] = *(const bf16x8*)(smem + aB + (s + 1) * 2048); p1[(s + 1) & 1] = *(const bf16x8*)(smem + aBo + (s + 1) * 2048);
        }
        SB();
        c0a = mfma32(asbf(w2[s]), p0[s & 1], c0a); c1a = mfma32(asbf(w2[s]), p1[s & 1], c1a);
        SB();
        if (s < 4) glds16(vo[1], sl + (12 + s) * 1024, lb + ((half * 16 + 12 + s) * 2 + tt) * 1024);
        if (s >= 12 && w == 0) { if (s == 12) LDW(64, 52, 55, w3, w3p); else if (s == 13) LDW(64, 55, 58, w3, w3p); else if (s == 14) LDW(64, 58, 61, w3, w3p); else LDW(64, 61, 64, w3, w3p); }
        SB();
      }
    }
#pragma unroll
    for (int q = 0; q < 4; ++q) *(f32x4*)(exw + q * 1024) = (f32x4){c1a[4 * q], c1a[4 * q + 1], c1a[4 * q + 2], c1a[4 * q + 3]};
    BAR();
    {
      f32x16 z = c0a;
#pragma unroll
      for (int q = 0; q < 4; ++q) { f32x4 p = *(const f32x4*)(exr + q * 1024);
#pragma unroll
        for (int j = 0; j < 4; ++j) z[4 * q + j] += p[j]; }
      f32x2 ssum2 = {0.f, 0.f}, ssq2 = {0.f, 0.f};
#pragma unroll
      for (int sp = 0; sp < 2; ++sp) {
        u32x4 o;
#pragma unroll
        for (int j = 0; j < 4; ++j) {
          int r = 8 * sp + 2 * j;
          u32 p = pk2v((f32x2){bflo(xb[sp][j]), bfhi(xb[sp][j])} + (f32x2){z[r], z[r + 1]});
          o[j] = p; const f32x2 a = {bflo(p), bfhi(p)};
          ssum2 += a; ssq2 = __builtin_elementwise_fma(a, a, ssq2);
        }
        x1r[sp] = o;
        st16(rs, WS_X1 + (T * 32 + s_out + sp) * 1024 + lane * 16, o);
      }
      float ssum = swap32_add(ssum2[0] + ssum2[1]), ssq = swap32_add(ssq2[0] + ssq2[1]);
      if (lane < 32) __builtin_amdgcn_raw_buffer_store_b64((u32x2){__float_as_uint(ssum), __float_as_uint(ssq)}, rs,
                                                           WS_ST + ((T * 32 + lane) * 16 + ct * 2 + nh) * 8, 0, 16);
    }
    WAITV(0);
    publish(ws, 3, me, tag);
  }
  u32x4 w4[56];
  {
    LDW(56, 0, 6, w4, w4p);
    {
      const bf16x8 onesv = {(__bf16)1.f, (__bf16)1.f, (__bf16)1.f, (__bf16)1.f, (__bf16)1.f, (__bf16)1.f, (__bf16)1.f, (__bf16)1.f};
      f32x4 ca[4] = {};
#pragma unroll
      for (int s = 0; s < 16; ++s)
#pragma unroll
        for (int i = 0; i < 4; ++i) ca[i] = mfma16(asbf(w3[i * 16 + s]), onesv, ca[i]);
      wait_flags(ws, 3, lane < 8 ? panel * 8 + lane : -1, w, tag);
      u32x4 stv[4][2];
      const char* slx = ws + WS_X1 + (size_t)(panel * 2) * 32 * 1024;
      auto stg3 = [&](int c, int i) { const int s16 = 8 * c + 2 * w + (i >> 1), tt = i & 1;
        glds16(lane * 16, slx + ((size_t)tt * 32 + s16) * 1024, lb + (s16 * 2 + tt) * 1024); };
#pragma unroll
      for (int c = 0; c < 3; ++c)
#pragma unroll
        for (int i = 0; i < 4; ++i) stg3(c, i);
      SB();
      f32x4 acc[14];
#pragma unroll
      for (int i = 0; i < 14; ++i) acc[i] = (f32x4){0.f, 0.f, 0.f, 0.f};
      const int g = lane >> 4;
      const u32 aBa = ((g >> 1) * 2) * 1024 + ((lane & 15) + 32 * (g & 1)) * 16 + (w & 1) * 1024, aBb = aBa ^ 1024;
      bf16x8 bq[2][4];
      auto ldb = [&](int s, int k) {
#pragma unroll
        for (int tk = 0; tk < 4; ++tk) bq[k][tk] = *(const bf16x8*)(smem + ((tk >> 1) ? aBb : aBa) + s * 4096 + (tk & 1) * 256);
      };
      WAITV(8); RAWBAR();
      ldb(0, 0);
#pragma unroll
      for (int s = 0; s < 16; ++s) {
        if (s + 1 < 16) {
          if (s + 1 == 4) { WAITV(7); RAWBAR(); } else if (s + 1 == 8) { WAITV(10); RAWBAR(); } else if (s + 1 == 12) { WAITV(17); RAWBAR(); }
          ldb(s + 1, (s + 1) & 1);
        }
        SB();
#pragma unroll
        for (int i = 0; i < 3; ++i)
#pragma unroll
          for (int tk = 0; tk < 4; ++tk) acc[i * 4 + tk] = mfma16(asbf(w3[i * 16 + s]), bq[s & 1][tk], acc[i * 4 + tk]);
        acc[12] = mfma16(asbf(w3[48 + s]), bq[s & 1][0], acc[12]); acc[13] = mfma16(asbf(w3[48 + s]), bq[s & 1][1], acc[13]);
        SB();
        if (s < 4) stg3(3, s);
        else if (s < 8) {
          const int tk = s - 4;
#pragma unroll
          for (int q = 0; q < 2; ++q)
            stv[tk][q] = ld16(rs, (panel * 64 + (tk ^ (2 * (w & 1))) * 16 + (lane & 15)) * 128 + (lane >> 4) * 32 + q * 16, WS_ST);
        } else if (s == 8) LDW(56, 6, 9, w4, w4p); else if (s == 9) LDW(56, 9, 12, w4, w4p); else if (s == 10) LDW(56, 12, 15, w4, w4p);
        else if (s == 11) LDW(56, 15, 18, w4, w4p); else if (s == 12) LDW(56, 18, 21, w4, w4p); else if (s == 13) LDW(56, 21, 24, w4, w4p);
        else if (s == 14) LDW(56, 24, 27, w4, w4p); else LDW(56, 27, 30, w4, w4p);
        SB();
      }
      float mean[4], rsc[4];
#pragma unroll
      for (int tk = 0; tk < 4; ++tk) {
        float a = __uint_as_float(stv[tk][0][0]) + __uint_as_float(stv[tk][0][2]) + __uint_as_float(stv[tk][1][0]) + __uint_as_float(stv[tk][1][2]);
        float q2 = __uint_as_float(stv[tk][0][1]) + __uint_as_float(stv[tk][0][3]) + __uint_as_float(stv[tk][1][1]) + __uint_as_float(stv[tk][1][3]);
        a = swap32_add(swap16_add(a)); q2 = swap32_add(swap16_add(q2));
        mean[tk] = a * (1.f / D);
        rsc[tk] = __builtin_amdgcn_rsqf(fmaxf(q2 * (1.f / D) - mean[tk] * mean[tk], 0.f) + 1e-5f);
      }
      auto gel = [&](int ti, int slot, int tk) -> u32x2 {
        const f32x4 c = ca[slot]; const float m = mean[tk], r = rsc[tk];
        return (u32x2){gelu_ln2(acc[ti][0], acc[ti][1], c[0], c[1], m, r), gelu_ln2(acc[ti][2], acc[ti][3], c[2], c[3], m, r)};
      };
      const int tkx = 2 * (w & 1);
      auto stpair = [&](u32x2 A, u32x2 B, int jA, int tkA, int jB, int tkB) {
        auto r0 = __builtin_amdgcn_permlane32_swap(A[0], B[0], false, false);
        auto r1 = __builtin_amdgcn_permlane32_swap(A[1], B[1], false, false);
        const int j = lane < 32 ? jA : jB, tk = (lane < 32 ? tkA : tkB) ^ tkx;
        st16(rs, WS_HH + ((panel * 2 + (tk >> 1)) * 112 + j) * 1024 + (16 * (tk & 1) + (lane & 15) + 32 * (g & 1)) * 16, (u32x4){r0[0], r1[0], r0[1], r1[1]});
      };
      const int j0 = ct * 14 + 3 * w, js = ct * 14 + 12 + (w >> 1);
      stpair(gel(0, 0, 0), gel(4, 1, 0), j0, 0, j0 + 1, 0); SB(); LDW(56, 30, 32, w4, w4p);
      stpair(gel(1, 0, 1), gel(5, 1, 1), j0, 1, j0 + 1, 1); SB(); LDW(56, 32, 34, w4, w4p);
      stpair(gel(2, 0, 2), gel(6, 1, 2), j0, 2, j0 + 1, 2); SB(); LDW(56, 34, 36, w4, w4p);
      stpair(gel(3, 0, 3), gel(7, 1, 3), j0, 3, j0 + 1, 3); SB(); LDW(56, 36, 38, w4, w4p);
      stpair(gel(8, 2, 0), gel(9, 2, 1), j0 + 2, 0, j0 + 2, 1); SB(); LDW(56, 38, 40, w4, w4p);
      stpair(gel(10, 2, 2), gel(11, 2, 3), j0 + 2, 2, j0 + 2, 3); SB(); LDW(56, 40, 42, w4, w4p);
      stpair(gel(12, 3, 0), gel(13, 3, 1), js, 0, js, 1);
      SB();
      LDW(56, 42, NW4A, w4, w4p);
      WAITV_T(NW4A - 42);
      publish(ws, 4, me, tag);
      LDW(56, NW4A, 56, w4, w4p);
    }
  }
  {
    wait_flags(ws, 4, lane < 8 ? panel * 8 + lane : -1, w, tag);
    const int half = w >> 1, tt = w & 1;
    const char* sl = ws + WS_HH + ((size_t)(panel * 2 + tt) * 112 + half * 56) * 1024;
    auto stage1 = [&](int c, int s4) { glds16(lane * 16, sl + (c * 4 + s4) * 1024, lb + (c & 7) * 16384 + ((half * 4 + s4) * 2 + tt) * 1024); };
#pragma unroll
    for (int c = 0; c < 3; ++c)
#pragma unroll
      for (int s4 = 0; s4 < 4; ++s4) stage1(c, s4);
    SB();
    f32x16 c0a = {}, c1a = {};
    const u32 aB = kh * 8 * 1024 + lane * 16 + tto * 1024, aBo = aB ^ 1024;
    bf16x8 p0[2], p1[2];
    WAITV(8); RAWBAR();
    p0[0] = *(const bf16x8*)(smem + aB); p1[0] = *(const bf16x8*)(smem + aBo);
#pragma unroll
    for (int s = 0; s < 56; ++s) {
      if (s + 1 < 56) {
        const int c = (s + 1) >> 2;
        if (((s + 1) & 3) == 0) {
          if (c + 2 < 14) WAITV(7); else if (c + 2 == 14) WAITV(4); else WAITV(0);
          RAWBAR();
        }
        const u32 sa = (c & 7) * 16384 + ((s + 1) & 3) * 2048;
        p0[(s + 1) & 1] = *(const bf16x8*)(smem + sa + aB); p1[(s + 1) & 1] = *(const bf16x8*)(smem + sa + aBo);
      }
      SB();
      c0a = mfma32(asbf(w4[s]), p0[s & 1], c0a); c1a = mfma32(asbf(w4[s]), p1[s & 1], c1a);
      SB();
      if ((s >> 2) + 3 < 14) stage1((s >> 2) + 3, s & 3);
      SB();
    }
#pragma unroll
    for (int q = 0; q < 4; ++q) *(f32x4*)(exw + q * 1024) = (f32x4){c1a[4 * q], c1a[4 * q + 1], c1a[4 * q + 2], c1a[4 * q + 3]};
    BAR();
    {
      f32x16 z = c0a;
#pragma unroll
      for (int q = 0; q < 4; ++q) { f32x4 p = *(const f32x4*)(exr + q * 1024);
#pragma unroll
        for (int j = 0; j < 4; ++j) z[4 * q + j] += p[j]; }
      u32x2 o[4];
#pragma unroll
      for (int qq = 0; qq < 4; ++qq) {
        const int sp = qq >> 1, jj = (qq & 1) * 2;
#pragma unroll
        for (int e = 0; e < 2; ++e) {
          const u32 xp = x1r[sp][jj + e], hp = hres[sp][jj + e];
          o[qq][e] = pk2v((f32x2){bflo(xp), bfhi(xp)} + (f32x2){bflo(hp), bfhi(hp)} + (f32x2){z[4 * qq + 2 * e], z[4 * qq + 2 * e + 1]});
        }
      }
      char* op = (char*)out + ((size_t)(T * 32 + (lane & 31)) * D + ct * 64 + nh * 32) * 2 + h * 16;
#pragma unroll
      for (int pr = 0; pr < 2; ++pr) {
        auto r0 = __builtin_amdgcn_permlane32_swap(o[2 * pr][0], o[2 * pr + 1][0], false, false);
        auto r1 = __builtin_amdgcn_permlane32_swap(o[2 * pr][1], o[2 * pr + 1][1], false, false);
        *(u32x4*)(op + pr * 32) = (u32x4){r0[0], r1[0], r0[1], r1[1]};
      }
    }
  }
}

extern "C" void kernel_launch(void* const* d_in, const int* in_sizes, int n_in, void* d_out, int out_size, void* d_ws, size_t ws_size,
                              hipStream_t stream) {
  (void)hipFuncSetAttribute((const void*)kmain, hipFuncAttributeMaxDynamicSharedMemorySize, LDS_BYTES);
  kmain<<<256, 256, LDS_BYTES, stream>>>((const u16*)d_in[0], (const u16*)d_in[1], (const u16*)d_in[2], (const u16*)d_in[3], (const u16*)d_in[4],
                                         (const u16*)d_in[5], (const u16*)d_in[6], (const u16*)d_in[8], (const u16*)d_in[9], (const u16*)d_in[10],
                                         (const u16*)d_in[11], (char*)d_ws, (u16*)d_out);
}
```

```cpp
#include <hip/hip_runtime.h>
#include <hip/hip_bf16.h>
#include <stdint.h>

typedef unsigned short u16;
typedef unsigned int u32;
typedef unsigned long long u64;
typedef u32 u32x2 __attribute__((ext_vector_type(2)));
typedef u32 u32x4 __attribute__((ext_vector_type(4)));
typedef float f32x2 __attribute__((ext_vector_type(2)));
typedef float f32x4 __attribute__((ext_vector_type(4)));
typedef float f32x16 __attribute__((ext_vector_type(16)));
typedef short s16x4 __attribute__((ext_vector_type(4)));
typedef __bf16 bf16x2 __attribute__((ext_vector_type(2)));
typedef __bf16 bf16x8 __attribute__((ext_vector_type(8)));
#define LAS __attribute__((address_space(3)))
#define GAS __attribute__((address_space(1)))
#define DI __device__ __forceinline__
typedef GAS u64 gu64;
#define RLX_AGENT __ATOMIC_RELAXED, __HIP_MEMORY_SCOPE_AGENT

constexpr int L = 2048, D = 512, NS = 128, F = 1792;
constexpr int KR_LEN = 2112;
constexpr int KR_OFF = 2063;

constexpr u32 WS_UX = 0;
constexpr u32 WS_HP = 4u << 20;
constexpr u32 WS_H1 = 6u << 20;
constexpr u32 WS_X1 = 9u << 20;
constexpr u32 WS_ST = 11u << 20;
constexpr u32 WS_HH = 12u << 20;
constexpr u32 WS_WT1 = 20u << 20;
constexpr u32 WS_WT2 = WS_WT1 + (512u << 10);
constexpr u32 WS_WT4 = 21u << 20;
constexpr u32 WS_WT3 = 23u << 20;
constexpr u32 WS_FL = 25u << 20;
constexpr u32 WS_SEED = WS_FL + 5 * 256 * 128;
constexpr u32 WS_SPAN = 32u << 20;

DI float bf2f(u32 v) { return __uint_as_float(v << 16); }
DI float bflo(u32 v) { return __uint_as_float(v << 16); }
DI float bfhi(u32 v) { return __uint_as_float(v & 0xffff0000u); }
DI u32 pk2(float lo, float hi) { f32x2 v = {lo, hi}; bf16x2 p = __builtin_convertvector(v, bf16x2); return __builtin_bit_cast(u32, p); }
DI u32 pk2v(f32x2 v) { return __builtin_bit_cast(u32, __builtin_convertvector(v, bf16x2)); }
DI float rbf(float f) { return bflo(pk2(f, 0.f) & 0xffffu); }
template <int CTRL, int RMASK>
DI float dppmov(float v) { return __int_as_float(__builtin_amdgcn_update_dpp(0, __float_as_int(v), CTRL, RMASK, 0xf, false)); }
DI float wave_sum(float v) {
  v += dppmov<0x111, 0xf>(v); v += dppmov<0x112, 0xf>(v); v += dppmov<0x114, 0xf>(v); v += dppmov<0x118, 0xf>(v);
  v += dppmov<0x142, 0xa>(v); v += dppmov<0x143, 0xc>(v);
  return __int_as_float(__builtin_amdgcn_readlane(__float_as_int(v), 63));
}
DI float swap32_add(float v) { auto r = __builtin_amdgcn_permlane32_swap(__float_as_uint(v), __float_as_uint(v), false, false); return __uint_as_float(r[0]) + __uint_as_float(r[1]); }
DI float swap16_add(float v) { auto r = __builtin_amdgcn_permlane16_swap(__float_as_uint(v), __float_as_uint(v), false, false); return __uint_as_float(r[0]) + __uint_as_float(r[1]); }
DI f32x2 gelu2(f32x2 z) {
  const float c0 = -2.f * 0.7978845608028654f * 1.4426950408889634f, c1 = c0 * 0.044715f;
  const f32x2 C0 = {c0, c0}, C1 = {c1, c1}, one = {1.f, 1.f};
  f32x2 a = z * __builtin_elementwise_fma(z * z, C1, C0);
  f32x2 e = {__builtin_amdgcn_exp2f(a[0]), __builtin_amdgcn_exp2f(a[1])};
  e = e + one;
  f32x2 r = {__builtin_amdgcn_rcpf(e[0]), __builtin_amdgcn_rcpf(e[1])};
  return z * r;
}
DI u32 gelu_ln2(float a0, float a1, float c0, float c1, float m, float r) {
  const f32x2 M = {-m, -m}, R = {r, r};
  f32x2 z = __builtin_elementwise_fma((f32x2){c0, c1}, M, (f32x2){a0, a1}) * R;
  return pk2v(gelu2(z));
}
DI void glds16(u32 voff, const void* sbase, u32 lds) {
  asm volatile("s_mov_b32 m0, %2\n\ts_nop 0\n\tglobal_load_lds_dwordx4 %0, %1 sc1" :: "v"(voff), "s"(sbase), "s"(lds) : "memory");
}
DI u32 lds_base(char* smem) { return (u32)(uintptr_t)(LAS char*)smem; }
#define WAITV(n) asm volatile("s_waitcnt vmcnt(" #n ")" ::: "memory")
#define WAITV_T(n) asm volatile("s_waitcnt vmcnt(%0)" :: "i"(n) : "memory")
#define SB() __builtin_amdgcn_sched_barrier(0)
#define RAWBAR() do { SB(); __builtin_amdgcn_s_barrier(); SB(); } while (0)
#define BAR() do { SB(); asm volatile("s_waitcnt lgkmcnt(0)" ::: "memory"); __builtin_amdgcn_s_barrier(); asm volatile("" ::: "memory"); SB(); } while (0)
DI bf16x8 tr_frag(char* smem, u32 a0, u32 a1) {
  s16x4 a = __builtin_amdgcn_ds_read_tr16_b64_v4i16((LAS s16x4*)(smem + a0));
  s16x4 b = __builtin_amdgcn_ds_read_tr16_b64_v4i16((LAS s16x4*)(smem + a1));
  return __builtin_bit_cast(bf16x8, __builtin_shufflevector(a, b, 0, 1, 2, 3, 4, 5, 6, 7));
}
DI f32x16 mfma32(bf16x8 a, bf16x8 b, f32x16 c) { return __builtin_amdgcn_mfma_f32_32x32x16_bf16(a, b, c, 0, 0, 0); }
DI f32x4 mfma16(bf16x8 a, bf16x8 b, f32x4 c) { return __builtin_amdgcn_mfma_f32_16x16x32_bf16(a, b, c, 0, 0, 0); }
DI bf16x8 asbf(u32x4 v) { return __builtin_bit_cast(bf16x8, v); }
typedef __amdgpu_buffer_rsrc_t rsrc_t;
DI u32x4 ld16(rsrc_t rs, u32 voff, u32 soff) { return __builtin_amdgcn_raw_buffer_load_b128(rs, voff, soff, 16); }
DI void st16(rsrc_t rs, u32 off, u32x4 v) { __builtin_amdgcn_raw_buffer_store_b128(v, rs, off, 0, 16); }
template <int N, int LO, int HI>
DI void ldw_(u32x4 (&a)[N], rsrc_t rs, u32 base, int lane) {
#pragma unroll
  for (int i = LO; i < HI; ++i) a[i] = ld16(rs, lane * 16, base + i * 1024);
  SB();
}
#define LDW(N, LO, HI, arr, base) ldw_<N, LO, HI>(arr, rs, base, lane)

DI u32 img128_srcoff(int lane) { return ((((lane >> 2) & 1) ^ ((lane >> 4) & 1)) * 64) + (lane & 3) * 16; }
DI u32 img128_traddr(int lane, int ct) {
  int h = lane >> 5, blk = (lane >> 4) & 1, q = (lane & 15) >> 2, p = lane & 3;
  return (4 * h + q) * 128 + ((ct ^ ((q >> 1) & 1)) * 64) + blk * 32 + p * 8;
}

struct KTab { u16 v[KR_LEN + 64]; };
constexpr int bf16_round_pos(int t) {
  if (t < 256) return t;
  int sh = t < 512 ? 1 : t < 1024 ? 2 : 3;
  int q = t >> sh, r = t & ((1 << sh) - 1), half = 1 << (sh - 1);
  if (r > half || (r == half && (q & 1))) ++q;
  return q << sh;
}
constexpr KTab make_ktab() {
  KTab t{};
  for (int i = 0; i < KR_LEN + 64; ++i) {
    int tau = KR_OFF - i; int idx = 1024;
    if (tau >= 0 && tau < L) {
      int p = bf16_round_pos(tau); int m = 0, col = 0;
      if (p < 256) { m = p & 7; col = p >> 3; }
      else if (p < 512) { m = 8 + ((p & 7) >> 1); col = (p - 256) >> 3; }
      else if (p < 1024) { m = 12 + 2 * ((p - 512) >> 8) + ((p >> 2) & 1); col = (p & 255) >> 3; }
      else { m = 16 + ((p - 1024) >> 8); col = (p & 255) >> 3; }
      idx = m * 32 + col;
    }
    t.v[i] = (u16)idx;
  }
  return t;
}
__device__ const KTab g_ktab = make_ktab();

struct cpx { float r, i; };
DI cpx cmul(cpx a, cpx b) { return {a.r * b.r - a.i * b.i, a.r * b.i + a.i * b.r}; }
DI cpx cexp_p(float ar, float ai_rev, float p) {
  float e = __builtin_amdgcn_exp2f(ar * p * 1.4426950408889634f);
  float rv = ai_rev * p; rv -= floorf(rv);
  return {e * __builtin_amdgcn_cosf(rv), e * __builtin_amdgcn_sinf(rv)};
}

constexpr int KC_STR = 4416, KC_CH = 8 * KC_STR;
constexpr int U_QS = 1568, U_CH = 4 * U_QS;
constexpr int LD_U = 2 * KC_CH;
constexpr int LD_X = LD_U + 2 * U_CH;
constexpr int LD_KV = 96 * 1024;
constexpr int LD_KS = LD_KV + 16384;
constexpr int LD_R = LD_KS + 2 * 4224;
constexpr int LD_LN = 132 * 1024;
constexpr int LD_EX = 128 * 1024;
constexpr int LD_SM = LD_EX + 16384;
constexpr int LD_CS1 = LD_SM + 1024, LD_CS3 = LD_CS1 + 1024;
constexpr int LD_PF = 148 * 1024 + 1024;
constexpr int LDS_BYTES = LD_PF + 1024;
static_assert(LD_R + 2 * (KR_LEN + 64) * 2 <= LD_LN, "lds");
static_assert(LD_LN + 16384 <= LDS_BYTES && LD_CS3 + 1024 <= LDS_BYTES, "lds");

constexpr unsigned SPIN_CAP = 1u << 18;
constexpr u64 TAG_INC = 0x9E3779B97F4A7C15ull;

DI void publish(char* ws, int phase, int id, u64 tag) {
  RAWBAR();
  if (threadIdx.x == 0) __hip_atomic_store((gu64*)(ws + WS_FL + (phase * 256 + id) * 128), tag, RLX_AGENT);
}
DI void wait_flags(char* ws, int phase, int idx, int w, u64 tag) {
  if (w == 0) {
    for (unsigned spins = 0; spins < SPIN_CAP; ++spins) {
      u64 f = tag;
      if (idx >= 0) f = __hip_atomic_load((gu64*)(ws + WS_FL + (phase * 256 + idx) * 128), RLX_AGENT);
      if (__all(f == tag)) break;
    }
  }
  RAWBAR();
}
DI void wait_all(char* ws, int phase, int w, int lane, u64 tag) {
  if (w == 0) {
    gu64* fp = (gu64*)(ws + WS_FL + (phase * 256 + lane) * 128);
    for (unsigned spins = 0; spins < SPIN_CAP; ++spins) {
      u64 f0 = __hip_atomic_load(fp, RLX_AGENT), f1 = __hip_atomic_load(fp + 64 * 16, RLX_AGENT);
      u64 f2 = __hip_atomic_load(fp + 128 * 16, RLX_AGENT), f3 = __hip_atomic_load(fp + 192 * 16, RLX_AGENT);
      if (__all(f0 == tag && f1 == tag && f2 == tag && f3 == tag)) break;
    }
  }
  RAWBAR();
}

#define NW3A 8
#define NW4A 46

__global__ void __launch_bounds__(256, 1) kmain(const u16* __restrict__ x, const u16* __restrict__ logA, const u16* __restrict__ Aim,
                                                const u16* __restrict__ Cr, const u16* __restrict__ Ci, const u16* __restrict__ logdt,
                                                const u16* __restrict__ dskip, const u16* __restrict__ w1g, const u16* __restrict__ w2g,
                                                const u16* __restrict__ w3g, const u16* __restrict__ w4g, char* ws, u16* out) {
  extern __shared__ __attribute__((aligned(16))) char smem[];
  const int tid = threadIdx.x, lane = tid & 63, w = __builtin_amdgcn_readfirstlane(tid >> 6);
  const int b = blockIdx.x, xcd = b & 7, idx = b >> 3;
  const u32 lb = lds_base(smem);
  const rsrc_t rs = __builtin_amdgcn_make_buffer_rsrc(ws, 0, WS_SPAN, 0x00020000);
  const int h = lane >> 5;
  {
    u64 pc; u32 clen;
    asm volatile("s_getpc_b64 %0\nkm_code_begin:\n\ts_mov_b32 %1, km_code_end-km_code_begin" : "=s"(pc), "=s"(clen));
    const u32 chunk = (idx * 4 + w) * 1024;
    if (chunk < clen) {
      const u32 vo = min(chunk + lane * 16, clen - 16) & ~15u;
      asm volatile("s_mov_b32 m0, %2\n\ts_nop 0\n\tglobal_load_lds_dwordx4 %0, %1" :: "v"(vo), "s"(pc & ~15ull), "s"(lb + LD_PF) : "memory");
    }
    if (w == 3) {
      const u32 vo = (lane & 15) << 21;
      asm volatile("s_mov_b32 m0, %2\n\ts_nop 0\n\tglobal_load_lds_dword %0, %1" :: "v"(vo), "s"(ws), "s"(lb + LD_PF) : "memory");
    }
  }
  u64 tag;
  { u64 sd = __hip_atomic_load((gu64*)(ws + WS_SEED), RLX_AGENT);
    u32 lo = __builtin_amdgcn_readfirstlane((u32)sd), hi = __builtin_amdgcn_readfirstlane((u32)(sd >> 32));
    tag = (((u64)hi << 32) | lo) + TAG_INC; }

  const int panel = xcd * 4 + (idx & 3), ct = idx >> 2, me = panel * 8 + ct;
  const int kh = w >> 1, nh = w & 1, tto = kh ^ nh;
  const u32 w1p = WS_WT1 + ((ct * 2 + nh) * 2 + kh) * 16384, w2p = WS_WT2 + ((ct * 2 + nh) * 2 + kh) * 16384;
  const u32 w3p = WS_WT3 + (ct * 4 + w) * 65536, w4p = WS_WT4 + ((ct * 2 + nh) * 2 + kh) * 56 * 1024;
  u32x4 w1[16], w2[16], w3[64];

  const int c0 = 2 * (xcd * 32 + idx);
  const int chl = w >> 1, ih = w & 1, ch = c0 + chl;
  {
    u32x4 xv[2];
#pragma unroll
    for (int r = 0; r < 2; ++r) xv[r] = *(const GAS u32x4*)(x + (size_t)(8 * b + 2 * w + r) * D + lane * 8);
    const int n = 64 * ih + lane;
    const u32 p_logA = logA[ch * NS + n], p_Aim = Aim[ch * NS + n], p_Cr = Cr[ch * NS + n], p_Ci = Ci[ch * NS + n], p_dt = logdt[ch];
    u32x4 ktab[3];
#pragma unroll
    for (int it = 0; it < 3; ++it) { int unit = it * 256 + tid; int u8 = unit >= KR_LEN / 8 ? unit - KR_LEN / 8 : unit; if (u8 >= KR_LEN / 8) u8 = 0; ktab[it] = *(const GAS u32x4*)(g_ktab.v + u8 * 8); }
    int u = w * 256 + b; const bool has_unit = u < 576;
    const u16* W = w1g; int N = D, ku = 0, nu = 0, kind = 0;
    if (u < 64) { W = w1g; ku = u >> 3; nu = u & 7; kind = 0; }
    else if (u < 128) { u -= 64; W = w2g; ku = u >> 3; nu = u & 7; kind = 1; }
    else if (u < 352) { u -= 128; W = w4g; ku = u >> 3; nu = u & 7; kind = 2; }
    else if (u < 576) { u -= 352; W = w3g; N = F; ku = u / 28; nu = u % 28; kind = 3; }
    u32x4 wv[8];
    if (has_unit) {
      const u16* src = W + (size_t)(ku * 64 + (lane >> 3)) * N + nu * 64 + (lane & 7) * 8;
#pragma unroll
      for (int i = 0; i < 8; ++i) wv[i] = *(const GAS u32x4*)(src + (size_t)i * 8 * N);
    }
    SB();
    {
      float uo[2][8];
#pragma unroll
      for (int r = 0; r < 2; ++r) {
        float v[8];
#pragma unroll
        for (int j = 0; j < 4; ++j) { v[2 * j] = bflo(xv[r][j]); v[2 * j + 1] = bfhi(xv[r][j]); }
        float s = 0;
#pragma unroll
        for (int j = 0; j < 8; ++j) s += v[j];
        float mean = wave_sum(s) * (1.f / D);
        float q = 0;
#pragma unroll
        for (int j = 0; j < 8; ++j) { float d = v[j] - mean; q += d * d; }
        float var = wave_sum(q) * (1.f / D);
        float mb = rbf(mean), sb = rbf(sqrtf(rbf(rbf(var) + 1e-5f)));
        float rsb = 1.f / sb;
#pragma unroll
        for (int j = 0; j < 8; ++j) uo[r][j] = rbf(v[j] - mb) * rsb;
      }
      const int sw = lane & 7;
#pragma unroll
      for (int j = 0; j < 8; ++j) {
        char* row = smem + LD_LN + (lane * 8 + j) * 32;
        const u32 x0 = xv[0][j >> 1], x1 = xv[1][j >> 1];
        *(u32*)(row + ((w ^ sw) * 4)) = pk2(uo[0][j], uo[1][j]);
        *(u32*)(row + (((4 + w) ^ sw) * 4)) = (j & 1) ? ((x0 >> 16) | (x1 & 0xffff0000u)) : ((x0 & 0xffffu) | (x1 << 16));
      }
    }
    BAR();
#pragma unroll
    for (int it = 0; it < 4; ++it) {
      const int unit = it * 256 + tid, cc = unit >> 1, half = unit & 1, s = (cc >> 3) & 7;
      u32x4 v = *(const u32x4*)(smem + LD_LN + cc * 32 + (((4 * half) ^ (s & 4)) * 4));
      if (s & 1) v = (u32x4){v[1], v[0], v[3], v[2]};
      if (s & 2) v = (u32x4){v[2], v[3], v[0], v[1]};
      st16(rs, WS_UX + b * 16384 + unit * 16, v);
    }
    if (has_unit) {
      char* img = smem + LD_KV + w * 8192; const u32 imgo = LD_KV + w * 8192;
      const int pc = lane & 7;
#pragma unroll
      for (int i = 0; i < 8; ++i) {
        const int r = i * 8 + (lane >> 3);
        const int pp = (kind == 3) ? ((((pc >> 1) ^ ((r >> 1) & 3)) << 1) | (pc & 1)) : ((((pc >> 2) ^ ((r >> 1) & 1)) << 2) | (pc & 3));
        *(u32x4*)(img + r * 128 + pp * 16) = wv[i];
      }
      if (kind != 3) {
        const int ksh = kind == 2 ? 56 : 16;
        const u32 dst = kind == 0 ? WS_WT1 : kind == 1 ? WS_WT2 : WS_WT4;
#pragma unroll
        for (int hn = 0; hn < 2; ++hn) {
          const u32 a = imgo + img128_traddr(lane, hn);
#pragma unroll
          for (int s4 = 0; s4 < 4; ++s4) {
            bf16x8 f = tr_frag(smem, a + s4 * 2048, a + s4 * 2048 + 1024);
            const int s = ku * 4 + s4; const int k2 = s >= ksh; const int sl = s - k2 * ksh;
            st16(rs, dst + (((nu * 2 + hn) * 2 + k2) * ksh + sl) * 1024 + lane * 16, __builtin_bit_cast(u32x4, f));
          }
        }
      } else {
        const int g = lane >> 4, q = (lane & 15) >> 2, p = lane & 3;
#pragma unroll
        for (int c16 = 0; c16 < 4; ++c16) {
          const int j = nu * 4 + c16; const int tc = j / 14, r14 = j % 14;
#pragma unroll
          for (int sp = 0; sp < 2; ++sp) {
            const int r0 = 32 * sp + 16 * (g >> 1) + 4 * (g & 1) + q, r1 = r0 + 8;
            bf16x8 f = tr_frag(smem, imgo + r0 * 128 + ((c16 ^ ((r0 >> 1) & 3)) * 32) + p * 8, imgo + r1 * 128 + ((c16 ^ ((r1 >> 1) & 3)) * 32) + p * 8);
            const u32 dst = WS_WT3 + tc * 4 * 65536 + (ku * 2 + sp) * 1024 + lane * 16;
            if (r14 < 12) st16(rs, dst + (r14 / 3) * 65536 + (r14 % 3) * 16384, __builtin_bit_cast(u32x4, f));
            else { const int w0 = 2 * (r14 - 12); st16(rs, dst + w0 * 65536 + 3 * 16384, __builtin_bit_cast(u32x4, f)); st16(rs, dst + (w0 + 1) * 65536 + 3 * 16384, __builtin_bit_cast(u32x4, f)); }
          }
        }
      }
    }
    if (b >= 252) {
      const u32 dst = WS_H1 + ((b & 2) ? 65u * 32 * 1024 : 0u) + (b & 1) * 16384 + tid * 16; const u32x4 z = {0, 0, 0, 0};
#pragma unroll
      for (int i = 0; i < 4; ++i) st16(rs, dst + i * 4096, z);
    }
    WAITV(0);
    publish(ws, 0, b, tag);
    const float dt = rbf(expf(bf2f(p_dt)));
    const float ar = -rbf(expf(bf2f(p_logA))) * dt;
    const float ai = bf2f(p_Aim) * dt * 0.15915494309189535f;
    const cpx c = {2.f * bf2f(p_Cr) * dt, 2.f * bf2f(p_Ci) * dt};
    char* tb = smem + w * (53 * 272);
    char* rowB = tb + lane * 4;
    char* rowA = tb + 32 * 272 + lane * 4;
    cpx w1c = cexp_p(ar, ai, 1.f), w8 = cexp_p(ar, ai, 8.f), e256 = cexp_p(ar, ai, 256.f);
    {
      cpx e0 = {1.f, 0.f}, e1 = cexp_p(ar, ai, 128.f);
#pragma unroll
      for (int col = 0; col < 16; ++col) {
        *(u32*)(rowB + col * 272) = pk2(e0.r, e0.i); *(u32*)(rowB + (16 + col) * 272) = pk2(e1.r, e1.i);
        e0 = cmul(e0, w8); e1 = cmul(e1, w8);
      }
      cpx e = c;
#pragma unroll
      for (int m = 0; m < 8; ++m) { *(u32*)(rowA + m * 272) = pk2(e.r, -e.i); e = cmul(e, w1c); }
      cpx w2c = cmul(w1c, w1c), w4c = cmul(w2c, w2c);
      cpx c256 = cmul(c, e256); e = c256;
#pragma unroll
      for (int m = 8; m < 12; ++m) { *(u32*)(rowA + m * 272) = pk2(e.r, -e.i); e = cmul(e, w2c); }
      cpx c512 = cmul(c256, e256), c768 = cmul(c512, e256);
      *(u32*)(rowA + 12 * 272) = pk2(c512.r, -c512.i); e = cmul(c512, w4c); *(u32*)(rowA + 13 * 272) = pk2(e.r, -e.i);
      *(u32*)(rowA + 14 * 272) = pk2(c768.r, -c768.i); e = cmul(c768, w4c); *(u32*)(rowA + 15 * 272) = pk2(e.r, -e.i);
      e = cmul(c768, e256);
#pragma unroll
      for (int m = 16; m < 21; ++m) { *(u32*)(rowA + m * 272) = pk2(e.r, -e.i); e = cmul(e, e256); }
    }
    f32x16 kacc = {};
    {
      const int m = lane & 31;
#pragma unroll
      for (int s = 0; s < 8; ++s) {
        bf16x8 af = *(const bf16x8*)(smem + w * (53 * 272) + (32 + m) * 272 + 32 * s + 16 * h);
        bf16x8 bf = *(const bf16x8*)(smem + w * (53 * 272) + m * 272 + 32 * s + 16 * h);
        kacc = mfma32(af, bf, kacc);
      }
    }
    {
      float* kv = (float*)(smem + LD_KV + w * 4096);
      const int col = lane & 31;
#pragma unroll
      for (int r = 0; r < 16; ++r) kv[((r & 3) + 8 * (r >> 2) + 4 * h) * 32 + col] = kacc[r];
    }
    BAR();
    {
#pragma unroll
      for (int it = 0; it < 2; ++it) {
        const int i4 = it * 256 + tid, c2 = i4 >> 8, e4 = i4 & 255;
        f32x4 a = *(const f32x4*)(smem + LD_KV + (2 * c2) * 4096 + e4 * 16), bq = *(const f32x4*)(smem + LD_KV + (2 * c2 + 1) * 4096 + e4 * 16);
        *(f32x4*)(smem + LD_KS + c2 * 4224 + e4 * 16) = a + bq;
      }
      if (tid < 2) *(float*)(smem + LD_KS + tid * 4224 + 4096) = 0.f;
    }
    BAR();
#pragma unroll
    for (int it = 0; it < 3; ++it) {
      const int unit = it * 256 + tid;
      if (unit < 2 * (KR_LEN / 8)) {
        const int c2 = unit >= KR_LEN / 8, u8 = unit - c2 * (KR_LEN / 8);
        const float* kv = (const float*)(smem + LD_KS + c2 * 4224);
        u32x4 o;
#pragma unroll
        for (int j = 0; j < 4; ++j) o[j] = pk2(kv[ktab[it][j] & 0xffffu], kv[ktab[it][j] >> 16]);
        *(u32x4*)(smem + LD_R + c2 * (KR_LEN + 64) * 2 + u8 * 16) = o;
      }
    }
    { const int c2 = tid >> 7, q = (tid >> 5) & 3, cc = tid & 31; *(u32x4*)(smem + LD_U + c2 * U_CH + q * U_QS + cc * 16) = (u32x4){0, 0, 0, 0}; }
    BAR();
#pragma unroll
    for (int it = 0; it < 3; ++it) {
      const int unit = it * 256 + tid;
      if (unit < 2 * (KR_LEN / 8)) {
        const int c2 = unit >= KR_LEN / 8, u8 = unit - c2 * (KR_LEN / 8);
        const char* src = smem + LD_R + c2 * (KR_LEN + 64) * 2 + u8 * 16;
        const u32x4 k0 = *(const u32x4*)src, k1 = *(const u32x4*)(src + 16);
        const u32 d[8] = {k0[0], k0[1], k0[2], k0[3], k1[0], k1[1], k1[2], k1[3]};
        u32 a[7];
#pragma unroll
        for (int k = 0; k < 7; ++k) a[k] = __builtin_amdgcn_alignbyte(d[k + 1], d[k], 2);
        char* dst = smem + c2 * KC_CH + u8 * 16;
#pragma unroll
        for (int k = 0; k < 4; ++k) {
          *(u32x4*)(dst + (7 - 2 * k) * KC_STR) = (u32x4){d[k], d[k + 1], d[k + 2], d[k + 3]};
          *(u32x4*)(dst + (6 - 2 * k) * KC_STR) = (u32x4){a[k], a[k + 1], a[k + 2], a[k + 3]};
        }
      }
    }
  }
  wait_all(ws, 0, w, lane, tag);
  if (b == 0 && tid == 0) __hip_atomic_store((gu64*)(ws + WS_SEED), tag, RLX_AGENT);
  {
    const float dsk = bf2f(dskip[ch]);
    {
      u32x4 gv[4];
#pragma unroll
      for (int i = 0; i < 4; ++i) gv[i] = ld16(rs, (64 * w + 16 * i + (lane >> 2)) * 16384 + (lane & 3) * 16, WS_UX + c0 * 32);
#pragma unroll
      for (int i = 0; i < 4; ++i) {
        const int tb = 64 * w + 16 * i + (lane >> 2), pc = lane & 3;
        *(u32x4*)(smem + ((pc & 1) ? LD_X : LD_U) + (pc >> 1) * U_CH + (tb & 3) * U_QS + (32 + (tb >> 2)) * 16) = gv[i];
      }
    }
    BAR();
    const int n = lane & 31;
    f32x16 acc0 = {}, acc1 = {};
    {
      const u32 abase = chl * KC_CH + (n & 7) * KC_STR + 16 * (257 + h - (n >> 3)) - 32 * (32 * ih - 1) - 32 * 95;
      const u32 ubase = LD_U + chl * U_CH + h * U_QS + (32 + n - 16 * ih) * 16 - 16 * 16;
      bf16x8 A1[3], A2[3], b0[3], b1[3];
      auto ld = [&](int ii) {
        const int k = ii % 3;
        A1[k] = *(const bf16x8*)(smem + abase + 32 * (95 - ii));
        A2[k] = *(const bf16x8*)(smem + abase + 32 * (95 - 64 - ii));
        const u32 pb = (ii & 1) ? (ubase + (16 - (ii - 1) / 2) * 16) : (ubase + 2 * U_QS + (16 - ii / 2) * 16);
        b0[k] = *(const bf16x8*)(smem + pb); b1[k] = *(const bf16x8*)(smem + pb + 32 * 16);
      };
      ld(0); ld(1);
#define CONV_STEP(ii) { if ((ii) + 2 < 32) ld((ii) + 2); SB(); \
        acc0 = mfma32(A1[(ii) % 3], b0[(ii) % 3], acc0); acc1 = mfma32(A1[(ii) % 3], b1[(ii) % 3], acc1); acc1 = mfma32(A2[(ii) % 3], b0[(ii) % 3], acc1); SB(); \
        if ((ii) < 16) LDW(16, (ii) < 16 ? (ii) : 0, (ii) < 16 ? (ii) + 1 : 0, w1, w1p); else LDW(16, (ii) >= 16 ? (ii) - 16 : 0, (ii) >= 16 ? (ii) - 15 : 0, w2, w2p); }
      CONV_STEP(0) CONV_STEP(1) CONV_STEP(2) CONV_STEP(3) CONV_STEP(4) CONV_STEP(5) CONV_STEP(6) CONV_STEP(7)
      CONV_STEP(8) CONV_STEP(9) CONV_STEP(10) CONV_STEP(11) CONV_STEP(12) CONV_STEP(13) CONV_STEP(14) CONV_STEP(15)
      CONV_STEP(16) CONV_STEP(17) CONV_STEP(18) CONV_STEP(19) CONV_STEP(20) CONV_STEP(21) CONV_STEP(22) CONV_STEP(23)
      CONV_STEP(24) CONV_STEP(25) CONV_STEP(26) CONV_STEP(27) CONV_STEP(28) CONV_STEP(29) CONV_STEP(30) CONV_STEP(31)
    }
    BAR();
    {
      f32x16 snd = ih ? acc0 : acc1;
      char* dst = smem + w * 4096 + lane * 16;
#pragma unroll
      for (int q = 0; q < 4; ++q) *(f32x4*)(dst + q * 1024) = (f32x4){snd[4 * q], snd[4 * q + 1], snd[4 * q + 2], snd[4 * q + 3]};
    }
    BAR();
    {
      f32x16 y = ih ? acc1 : acc0;
      const char* src = smem + (w ^ 1) * 4096 + lane * 16;
      u32x2 o[4];
#pragma unroll
      for (int q = 0; q < 4; ++q) {
        const f32x4 p = *(const f32x4*)(src + q * 1024);
        const u32 io = chl * U_CH + q * U_QS + (32 + 32 * ih + n) * 16 + 8 * h;
        const u32x2 uu = *(const u32x2*)(smem + LD_U + io), xx = *(const u32x2*)(smem + LD_X + io);
#pragma unroll
        for (int e = 0; e < 2; ++e) {
          const float y0 = rbf(y[4 * q + 2 * e] + p[2 * e]), y1 = rbf(y[4 * q + 2 * e + 1] + p[2 * e + 1]);
          const float t0 = rbf(y0 + rbf(bflo(uu[e]) * dsk)), t1 = rbf(y1 + rbf(bfhi(uu[e]) * dsk));
          o[q][e] = pk2(bflo(xx[e]) + t0, bfhi(xx[e]) + t1);
        }
      }
      const u32 hp = WS_HP + (16 * ih + (n >> 1)) * 65536 + ch * 128 + (32 * (n & 1) + 8 * h) * 2;
#pragma unroll
      for (int pr = 0; pr < 2; ++pr) {
        auto r0 = __builtin_amdgcn_permlane32_swap(o[2 * pr][0], o[2 * pr + 1][0], false, false);
        auto r1 = __builtin_amdgcn_permlane32_swap(o[2 * pr][1], o[2 * pr + 1][1], false, false);
        st16(rs, hp + pr * 32, (u32x4){r0[0], r1[0], r0[1], r1[1]});
      }
    }
    SB();
    LDW(64, 0, NW3A, w3, w3p);
    WAITV_T(NW3A);
    publish(ws, 1, b, tag);
    if (w != 0) LDW(64, NW3A, 40, w3, w3p); else LDW(64, NW3A, 20, w3, w3p);
    {
      const bf16x8 onesv = {(__bf16)1.f, (__bf16)1.f, (__bf16)1.f, (__bf16)1.f, (__bf16)1.f, (__bf16)1.f, (__bf16)1.f, (__bf16)1.f};
      f32x16 cacc = {};
#pragma unroll
      for (int s = 0; s < 16; ++s) cacc = mfma32(asbf(w1[s]), onesv, cacc);
      if ((lane & 31) == 0) {
        float* dst = (float*)(smem + LD_CS1) + (kh * 2 + nh) * 32 + 4 * h;
#pragma unroll
        for (int q = 0; q < 4; ++q) *(f32x4*)(dst + 8 * q) = (f32x4){cacc[4 * q], cacc[4 * q + 1], cacc[4 * q + 2], cacc[4 * q + 3]};
      }
    }
    wait_all(ws, 1, w, lane, tag);
  }

  const int T = panel * 2 + tto; const int s_out = ct * 4 + nh * 2;
  char* exw = smem + LD_EX + w * 4096 + lane * 16;
  const char* exr = smem + LD_EX + (w ^ 2) * 4096 + lane * 16;
  u32x4 hres[2], xb[2], x1r[2];
  {
    {
      const int half = w >> 1, sub = w & 1;
      const char* sl = ws + WS_HP + (size_t)panel * 65536 + (half * 256 + sub * 32) * 128;
      const u32 vo = (lane >> 3) * 128 + img128_srcoff(lane);
#pragma unroll
      for (int c = 0; c < 3; ++c)
#pragma unroll
        for (int i = 0; i < 4; ++i) glds16(vo, sl + (c * 64 + i * 8) * 128, lb + (half * 256 + sub * 32 + c * 64 + i * 8) * 128);
      SB();
      f32x16 acc0 = {}, acc1 = {};
      float sa0 = 0.f, sa1 = 0.f, sq0 = 0.f, sq1 = 0.f;
      const u32 aB0 = kh * 256 * 128 + img128_traddr(lane, tto), aB1 = kh * 256 * 128 + img128_traddr(lane, tto ^ 1);
      const bf16x2 ones = {(__bf16)1.0f, (__bf16)1.0f};
      bf16x8 b0[2], b1[2];
      WAITV(8); RAWBAR();
      b0[0] = tr_frag(smem, aB0, aB0 + 1024); b1[0] = tr_frag(smem, aB1, aB1 + 1024);
#pragma unroll
      for (int s = 0; s < 16; ++s) {
        if (s + 1 < 16) {
          if (s + 1 == 4) { WAITV(7); RAWBAR(); } else if (s + 1 == 8) { WAITV(4); RAWBAR(); } else if (s + 1 == 12) { WAITV(0); RAWBAR(); }
          b0[(s + 1) & 1] = tr_frag(smem, aB0 + (s + 1) * 2048, aB0 + (s + 1) * 2048 + 1024);
          b1[(s + 1) & 1] = tr_frag(smem, aB1 + (s + 1) * 2048, aB1 + (s + 1) * 2048 + 1024);
        }
        SB();
        acc0 = mfma32(asbf(w1[s]), b0[s & 1], acc0); acc1 = mfma32(asbf(w1[s]), b1[s & 1], acc1);
        SB();
        if (s < 4) glds16(vo, sl + (3 * 64 + s * 8) * 128, lb + (half * 256 + sub * 32 + 3 * 64 + s * 8) * 128);
        { const bf16x8 st = b0[s & 1];
          const bf16x2 p0 = {st[0], st[1]}, p1 = {st[2], st[3]}, p2 = {st[4], st[5]}, p3 = {st[6], st[7]};
          sa0 = __builtin_amdgcn_fdot2_f32_bf16(p0, ones, sa0, false); sq0 = __builtin_amdgcn_fdot2_f32_bf16(p0, p0, sq0, false);
          sa1 = __builtin_amdgcn_fdot2_f32_bf16(p1, ones, sa1, false); sq1 = __builtin_amdgcn_fdot2_f32_bf16(p1, p1, sq1, false);
          sa0 = __builtin_amdgcn_fdot2_f32_bf16(p2, ones, sa0, false); sq0 = __builtin_amdgcn_fdot2_f32_bf16(p2, p2, sq0, false);
          sa1 = __builtin_amdgcn_fdot2_f32_bf16(p3, ones, sa1, false); sq1 = __builtin_amdgcn_fdot2_f32_bf16(p3, p3, sq1, false); }
        if (s >= 12 && w == 0) { if (s == 12) LDW(64, 20, 25, w3, w3p); else if (s == 13) LDW(64, 25, 30, w3, w3p); else if (s == 14) LDW(64, 30, 35, w3, w3p); else LDW(64, 35, 40, w3, w3p); }
        SB();
      }
      float ssum = sa0 + sa1, ssq = sq0 + sq1;
#pragma unroll
      for (int q = 0; q < 4; ++q) *(f32x4*)(exw + q * 1024) = (f32x4){acc1[4 * q], acc1[4 * q + 1], acc1[4 * q + 2], acc1[4 * q + 3]};
      ssum = swap32_add(ssum); ssq = swap32_add(ssq);
      if (lane < 32) *(f32x2*)(smem + LD_SM + ((kh * 2 + tto) * 32 + lane) * 8) = (f32x2){ssum, ssq};
      BAR();
      f32x16 z = acc0;
#pragma unroll
      for (int q = 0; q < 4; ++q) { f32x4 p = *(const f32x4*)(exr + q * 1024);
#pragma unroll
        for (int j = 0; j < 4; ++j) z[4 * q + j] += p[j]; }
      f32x4 cs[4];
#pragma unroll
      for (int q = 0; q < 4; ++q) cs[q] = *(const f32x4*)(smem + LD_CS1 + ((0 * 2 + nh) * 32 + 8 * q + 4 * h) * 4) + *(const f32x4*)(smem + LD_CS1 + ((1 * 2 + nh) * 32 + 8 * q + 4 * h) * 4);
      f32x2 s0 = *(const f32x2*)(smem + LD_SM + ((0 * 2 + tto) * 32 + (lane & 31)) * 8);
      f32x2 s1 = *(const f32x2*)(smem + LD_SM + ((1 * 2 + tto) * 32 + (lane & 31)) * 8);
      const float mean = (s0[0] + s1[0]) * (1.f / D);
      const float var = fmaxf((s0[1] + s1[1]) * (1.f / D) - mean * mean, 0.f);
      const float rs1 = __builtin_amdgcn_rsqf(var + 1e-5f);
      const float var2 = var * rs1 * rs1;
      const float rsc = rs1 * __builtin_amdgcn_rsqf(var2 + 1e-5f);
      const u32 ah = img128_traddr(lane, tto);
      const f32x2 R1 = {rs1, rs1}, MR = {-mean * rs1, -mean * rs1};
#pragma unroll
      for (int sp = 0; sp < 2; ++sp) {
        const int s = s_out + sp;
        bf16x8 hf = tr_frag(smem, ah + s * 2048, ah + s * 2048 + 1024);
        u32x4 hv = __builtin_bit_cast(u32x4, hf);
        u32x4 o;
#pragma unroll
        for (int j = 0; j < 4; ++j) {
          int r = 8 * sp + 2 * j;
          o[j] = gelu_ln2(z[r], z[r + 1], cs[r >> 2][r & 3], cs[(r + 1) >> 2][(r + 1) & 3], mean, rsc);
          xb[sp][j] = pk2v(__builtin_elementwise_fma((f32x2){bflo(hv[j]), bfhi(hv[j])}, R1, MR));
        }
        hres[sp] = hv;
        st16(rs, WS_H1 + 32 * 1024 + (T * 32 + s) * 1024 + lane * 16, o);
      }
    }
    SB();
    LDW(64, 40, 46, w3, w3p);
    WAITV(6);
    publish(ws, 2, me, tag);
    if (w != 0) LDW(64, 46, 64, w3, w3p); else LDW(64, 46, 52, w3, w3p);
  }
  {
    {
      int ix = -1;
      if (lane < 8) ix = panel * 8 + lane;
      else if (lane < 12) { if (panel > 0) ix = (panel - 1) * 8 + ((lane & 1) | ((lane & 2) << 1)); }
      else if (lane < 16) { if (panel < 31) ix = (panel + 1) * 8 + 2 + ((lane & 1) | ((lane & 2) << 1)); }
      wait_flags(ws, 2, ix, w, tag);
    }
    f32x16 c0a = {}, c1a = {};
    {
      const int half = w >> 1, tt = w & 1;
      u32 vo[2];
#pragma unroll
      for (int gg = 0; gg < 2; ++gg) {
        int sh = half ? (gg ? 2 : -2) : (gg ? 1 : -1);
        int tok = tt * 32 + (lane & 31) + sh + 32;
        vo[gg] = (tok >> 5) * 32 * 1024 + ((tok & 31) + 32 * (lane >> 5)) * 16;
      }
      const char* sl = ws + WS_H1 + (size_t)(panel * 2) * 32 * 1024 + (size_t)half * 16 * 1024;
#pragma unroll
      for (int s = 0; s < 12; ++s) glds16(vo[s >> 3], sl + s * 1024, lb + ((half * 16 + s) * 2 + tt) * 1024);
      SB();
      const u32 aB = kh * 32 * 1024 + lane * 16 + tto * 1024, aBo = aB ^ 1024;
      bf16x8 p0[2], p1[2];
      WAITV(8); RAWBAR();
      p0[0] = *(const bf16x8*)(smem + aB); p1[0] = *(const bf16x8*)(smem + aBo);
#pragma unroll
      for (int s = 0; s < 16; ++s) {
        if (s + 1 < 16) {
          if (s + 1 == 4) { WAITV(7); RAWBAR(); } else if (s + 1 == 8) { WAITV(4); RAWBAR(); } else if (s + 1 == 12) { WAITV(0); RAWBAR(); }
          p0[(s + 1) & 1] = *(const bf16x8*)(smem + aB + (s + 1) * 2048); p1[(s + 1) & 1] = *(const bf16x8*)(smem + aBo + (s + 1) * 2048);
        }
        SB();
        c0a = mfma32(asbf(w2[s]), p0[s & 1], c0a); c1a = mfma32(asbf(w2[s]), p1[s & 1], c1a);
        SB();
        if (s < 4) glds16(vo[1], sl + (12 + s) * 1024, lb + ((half * 16 + 12 + s) * 2 + tt) * 1024);
        if (s >= 12 && w == 0) { if (s == 12) LDW(64, 52, 55, w3, w3p); else if (s == 13) LDW(64, 55, 58, w3, w3p); else if (s == 14) LDW(64, 58, 61, w3, w3p); else LDW(64, 61, 64, w3, w3p); }
        SB();
      }
    }
#pragma unroll
    for (int q = 0; q < 4; ++q) *(f32x4*)(exw + q * 1024) = (f32x4){c1a[4 * q], c1a[4 * q + 1], c1a[4 * q + 2], c1a[4 * q + 3]};
    BAR();
    {
      f32x16 z = c0a;
#pragma unroll
      for (int q = 0; q < 4; ++q) { f32x4 p = *(const f32x4*)(exr + q * 1024);
#pragma unroll
        for (int j = 0; j < 4; ++j) z[4 * q + j] += p[j]; }
      f32x2 ssum2 = {0.f, 0.f}, ssq2 = {0.f, 0.f};
#pragma unroll
      for (int sp = 0; sp < 2; ++sp) {
        u32x4 o;
#pragma unroll
        for (int j = 0; j < 4; ++j) {
          int r = 8 * sp + 2 * j;
          u32 p = pk2v((f32x2){bflo(xb[sp][j]), bfhi(xb[sp][j])} + (f32x2){z[r], z[r + 1]});
          o[j] = p; const f32x2 a = {bflo(p), bfhi(p)};
          ssum2 += a; ssq2 = __builtin_elementwise_fma(a, a, ssq2);
        }
        x1r[sp] = o;
        st16(rs, WS_X1 + (T * 32 + s_out + sp) * 1024 + lane * 16, o);
      }
      float ssum = swap32_add(ssum2[0] + ssum2[1]), ssq = swap32_add(ssq2[0] + ssq2[1]);
      if (lane < 32) __builtin_amdgcn_raw_buffer_store_b64((u32x2){__float_as_uint(ssum), __float_as_uint(ssq)}, rs,
                                                           WS_ST + ((T * 32 + lane) * 16 + ct * 2 + nh) * 8, 0, 16);
    }
    WAITV(0);
    publish(ws, 3, me, tag);
  }
  u32x4 w4[56];
  {
    LDW(56, 0, 6, w4, w4p);
    {
      const bf16x8 onesv = {(__bf16)1.f, (__bf16)1.f, (__bf16)1.f, (__bf16)1.f, (__bf16)1.f, (__bf16)1.f, (__bf16)1.f, (__bf16)1.f};
      f32x4 ca[4] = {};
#pragma unroll
      for (int s = 0; s < 16; ++s)
#pragma unroll
        for (int i = 0; i < 4; ++i) ca[i] = mfma16(asbf(w3[i * 16 + s]), onesv, ca[i]);
      wait_flags(ws, 3, lane < 8 ? panel * 8 + lane : -1, w, tag);
      u32x4 stv[4][2];
      const char* slx = ws + WS_X1 + (size_t)(panel * 2) * 32 * 1024;
      auto stg3 = [&](int c, int i) { const int s16 = 8 * c + 2 * w + (i >> 1), tt = i & 1;
        glds16(lane * 16, slx + ((size_t)tt * 32 + s16) * 1024, lb + (s16 * 2 + tt) * 1024); };
#pragma unroll
      for (int c = 0; c < 3; ++c)
#pragma unroll
        for (int i = 0; i < 4; ++i) stg3(c, i);
      SB();
      f32x4 acc[14];
#pragma unroll
      for (int i = 0; i < 14; ++i) acc[i] = (f32x4){0.f, 0.f, 0.f, 0.f};
      const int g = lane >> 4;
      const u32 aBa = ((g >> 1) * 2) * 1024 + ((lane & 15) + 32 * (g & 1)) * 16 + (w & 1) * 1024, aBb = aBa ^ 1024;
      bf16x8 bq[2][4];
      auto ldb = [&](int s, int k) {
#pragma unroll
        for (int tk = 0; tk < 4; ++tk) bq[k][tk] = *(const bf16x8*)(smem + ((tk >> 1) ? aBb : aBa) + s * 4096 + (tk & 1) * 256);
      };
      WAITV(8); RAWBAR();
      ldb(0, 0);
#pragma unroll
      for (int s = 0; s < 16; ++s) {
        if (s + 1 < 16) {
          if (s + 1 == 4) { WAITV(7); RAWBAR(); } else if (s + 1 == 8) { WAITV(10); RAWBAR(); } else if (s + 1 == 12) { WAITV(17); RAWBAR(); }
          ldb(s + 1, (s + 1) & 1);
        }
        SB();
#pragma unroll
        for (int i = 0; i < 3; ++i)
#pragma unroll
          for (int tk = 0; tk < 4; ++tk) acc[i * 4 + tk] = mfma16(asbf(w3[i * 16 + s]), bq[s & 1][tk], acc[i * 4 + tk]);
        acc[12] = mfma16(asbf(w3[48 + s]), bq[s & 1][0], acc[12]); acc[13] = mfma16(asbf(w3[48 + s]), bq[s & 1][1], acc[13]);
        SB();
        if (s < 4) stg3(3, s);
        else if (s < 8) {
          const int tk = s - 4;
#pragma unroll
          for (int q = 0; q < 2; ++q)
            stv[tk][q] = ld16(rs, (panel * 64 + (tk ^ (2 * (w & 1))) * 16 + (lane & 15)) * 128 + (lane >> 4) * 32 + q * 16, WS_ST);
        } else if (s == 8) LDW(56, 6, 9, w4, w4p); else if (s == 9) LDW(56, 9, 12, w4, w4p); else if (s == 10) LDW(56, 12, 15, w4, w4p);
        else if (s == 11) LDW(56, 15, 18, w4, w4p); else if (s == 12) LDW(56, 18, 21, w4, w4p); else if (s == 13) LDW(56, 21, 24, w4, w4p);
        else if (s == 14) LDW(56, 24, 27, w4, w4p); else LDW(56, 27, 30, w4, w4p);
        SB();
      }
      float mean[4], rsc[4];
#pragma unroll
      for (int tk = 0; tk < 4; ++tk) {
        float a = __uint_as_float(stv[tk][0][0]) + __uint_as_float(stv[tk][0][2]) + __uint_as_float(stv[tk][1][0]) + __uint_as_float(stv[tk][1][2]);
        float q2 = __uint_as_float(stv[tk][0][1]) + __uint_as_float(stv[tk][0][3]) + __uint_as_float(stv[tk][1][1]) + __uint_as_float(stv[tk][1][3]);
        a = swap32_add(swap16_add(a)); q2 = swap32_add(swap16_add(q2));
        mean[tk] = a * (1.f / D);
        rsc[tk] = __builtin_amdgcn_rsqf(fmaxf(q2 * (1.f / D) - mean[tk] * mean[tk], 0.f) + 1e-5f);
      }
      auto gel = [&](int ti, int slot, int tk) -> u32x2 {
        const f32x4 c = ca[slot]; const float m = mean[tk], r = rsc[tk];
        return (u32x2){gelu_ln2(acc[ti][0], acc[ti][1], c[0], c[1], m, r), gelu_ln2(acc[ti][2], acc[ti][3], c[2], c[3], m, r)};
      };
      const int tkx = 2 * (w & 1);
      auto stpair = [&](u32x2 A, u32x2 B, int jA, int tkA, int jB, int tkB) {
        auto r0 = __builtin_amdgcn_permlane32_swap(A[0], B[0], false, false);
        auto r1 = __builtin_amdgcn_permlane32_swap(A[1], B[1], false, false);
        const int j = lane < 32 ? jA : jB, tk = (lane < 32 ? tkA : tkB) ^ tkx;
        st16(rs, WS_HH + ((panel * 2 + (tk >> 1)) * 112 + j) * 1024 + (16 * (tk & 1) + (lane & 15) + 32 * (g & 1)) * 16, (u32x4){r0[0], r1[0], r0[1], r1[1]});
      };
      const int j0 = ct * 14 + 3 * w, js = ct * 14 + 12 + (w >> 1);
      stpair(gel(0, 0, 0), gel(4, 1, 0), j0, 0, j0 + 1, 0); SB(); LDW(56, 30, 32, w4, w4p);
      stpair(gel(1, 0, 1), gel(5, 1, 1), j0, 1, j0 + 1, 1); SB(); LDW(56, 32, 34, w4, w4p);
      stpair(gel(2, 0, 2), gel(6, 1, 2), j0, 2, j0 + 1, 2); SB(); LDW(56, 34, 36, w4, w4p);
      stpair(gel(3, 0, 3), gel(7, 1, 3), j0, 3, j0 + 1, 3); SB(); LDW(56, 36, 38, w4, w4p);
      stpair(gel(8, 2, 0), gel(9, 2, 1), j0 + 2, 0, j0 + 2, 1); SB(); LDW(56, 38, 40, w4, w4p);
      stpair(gel(10, 2, 2), gel(11, 2, 3), j0 + 2, 2, j0 + 2, 3); SB(); LDW(56, 40, 42, w4, w4p);
      stpair(gel(12, 3, 0), gel(13, 3, 1), js, 0, js, 1);
      SB();
      LDW(56, 42, NW4A, w4, w4p);
      WAITV_T(NW4A - 42);
      publish(ws, 4, me, tag);
      LDW(56, NW4A, 56, w4, w4p);
    }
  }
  {
    wait_flags(ws, 4, lane < 8 ? panel * 8 + lane : -1, w, tag);
    const int half = w >> 1, tt = w & 1;
    const char* sl = ws + WS_HH + ((size_t)(panel * 2 + tt) * 112 + half * 56) * 1024;
    auto stage1 = [&](int c, int s4) { glds16(lane * 16, sl + (c * 4 + s4) * 1024, lb + (c & 7) * 16384 + ((half * 4 + s4) * 2 + tt) * 1024); };
#pragma unroll
    for (int c = 0; c < 3; ++c)
#pragma unroll
      for (int s4 = 0; s4 < 4; ++s4) stage1(c, s4);
    SB();
    f32x16 c0a = {}, c1a = {};
    const u32 aB = kh * 8 * 1024 + lane * 16 + tto * 1024, aBo = aB ^ 1024;
    bf16x8 p0[2], p1[2];
    WAITV(8); RAWBAR();
    p0[0] = *(const bf16x8*)(smem + aB); p1[0] = *(const bf16x8*)(smem + aBo);
#pragma unroll
    for (int s = 0; s < 56; ++s) {
      if (s + 1 < 56) {
        const int c = (s + 1) >> 2;
        if (((s + 1) & 3) == 0) {
          if (c + 2 < 14) WAITV(7); else if (c + 2 == 14) WAITV(4); else WAITV(0);
          RAWBAR();
        }
        const u32 sa = (c & 7) * 16384 + ((s + 1) & 3) * 2048;
        p0[(s + 1) & 1] = *(const bf16x8*)(smem + sa + aB); p1[(s + 1) & 1] = *(const bf16x8*)(smem + sa + aBo);
      }
      SB();
      c0a = mfma32(asbf(w4[s]), p0[s & 1], c0a); c1a = mfma32(asbf(w4[s]), p1[s & 1], c1a);
      SB();
      if ((s >> 2) + 3 < 14) stage1((s >> 2) + 3, s & 3);
      SB();
    }
#pragma unroll
    for (int q = 0; q < 4; ++q) *(f32x4*)(exw + q * 1024) = (f32x4){c1a[4 * q], c1a[4 * q + 1], c1a[4 * q + 2], c1a[4 * q + 3]};
    BAR();
    {
      f32x16 z = c0a;
#pragma unroll
      for (int q = 0; q < 4; ++q) { f32x4 p = *(const f32x4*)(exr + q * 1024);
#pragma unroll
        for (int j = 0; j < 4; ++j) z[4 * q + j] += p[j]; }
      u32x2 o[4];
#pragma unroll
      for (int qq = 0; qq < 4; ++qq) {
        const int sp = qq >> 1, jj = (qq & 1) * 2;
#pragma unroll
        for (int e = 0; e < 2; ++e) {
          const u32 xp = x1r[sp][jj + e], hp = hres[sp][jj + e];
          o[qq][e] = pk2v((f32x2){bflo(xp), bfhi(xp)} + (f32x2){bflo(hp), bfhi(hp)} + (f32x2){z[4 * qq + 2 * e], z[4 * qq + 2 * e + 1]});
        }
      }
      char* op = (char*)out + ((size_t)(T * 32 + (lane & 31)) * D + ct * 64 + nh * 32) * 2 + h * 16;
#pragma unroll
      for (int pr = 0; pr < 2; ++pr) {
        auto r0 = __builtin_amdgcn_permlane32_swap(o[2 * pr][0], o[2 * pr + 1][0], false, false);
        auto r1 = __builtin_amdgcn_permlane32_swap(o[2 * pr][1], o[2 * pr + 1][1], false, false);
        *(u32x4*)(op + pr * 32) = (u32x4){r0[0], r1[0], r0[1], r1[1]};
      }
    }
  }
  asm volatile("km_code_end:");
}

extern "C" void kernel_launch(void* const* d_in, const int* in_sizes, int n_in, void* d_out, int out_size, void* d_ws, size_t ws_size,
                              hipStream_t stream) {
  (void)hipFuncSetAttribute((const void*)kmain, hipFuncAttributeMaxDynamicSharedMemorySize, LDS_BYTES);
  kmain<<<256, 256, LDS_BYTES, stream>>>((const u16*)d_in[0], (const u16*)d_in[1], (const u16*)d_in[2], (const u16*)d_in[3], (const u16*)d_in[4],
                                         (const u16*)d_in[5], (const u16*)d_in[6], (const u16*)d_in[8], (const u16*)d_in[9], (const u16*)d_in[10],
                                         (const u16*)d_in[11], (char*)d_ws, (u16*)d_out);
}
```

```cpp
#include <hip/hip_runtime.h>
#include <hip/hip_bf16.h>
#include <stdint.h>

typedef unsigned short u16;
typedef unsigned int u32;
typedef unsigned long long u64;
typedef u32 u32x2 __attribute__((ext_vector_type(2)));
typedef u32 u32x4 __attribute__((ext_vector_type(4)));
typedef float f32x2 __attribute__((ext_vector_type(2)));
typedef float f32x4 __attribute__((ext_vector_type(4)));
typedef float f32x16 __attribute__((ext_vector_type(16)));
typedef short s16x4 __attribute__((ext_vector_type(4)));
typedef __bf16 bf16x2 __attribute__((ext_vector_type(2)));
typedef __bf16 bf16x8 __attribute__((ext_vector_type(8)));
#define LAS __attribute__((address_space(3)))
#define GAS __attribute__((address_space(1)))
#define DI __device__ __forceinline__
typedef GAS u64 gu64;
#define RLX_AGENT __ATOMIC_RELAXED, __HIP_MEMORY_SCOPE_AGENT

constexpr int L = 2048, D = 512, NS = 128, F = 1792;
constexpr int KR_LEN = 2112;
constexpr int KR_OFF = 2063;

constexpr u32 WS_UX = 0;
constexpr u32 WS_HP = 4u << 20;
constexpr u32 WS_H1 = 6u << 20;
constexpr u32 WS_X1 = 9u << 20;
constexpr u32 WS_ST = 11u << 20;
constexpr u32 WS_HH = 12u << 20;
constexpr u32 WS_WT1 = 20u << 20;
constexpr u32 WS_WT2 = WS_WT1 + (512u << 10);
constexpr u32 WS_WT4 = 21u << 20;
constexpr u32 WS_WT3 = 23u << 20;
constexpr u32 WS_FL = 25u << 20;
constexpr u32 WS_SEED = WS_FL + 7 * 256 * 128;
constexpr u32 WS_SPAN = 32u << 20;

DI float bf2f(u32 v) { return __uint_as_float(v << 16); }
DI float bflo(u32 v) { return __uint_as_float(v << 16); }
DI float bfhi(u32 v) { return __uint_as_float(v & 0xffff0000u); }
DI u32 pk2(float lo, float hi) { f32x2 v = {lo, hi}; bf16x2 p = __builtin_convertvector(v, bf16x2); return __builtin_bit_cast(u32, p); }
DI u32 pk2v(f32x2 v) { return __builtin_bit_cast(u32, __builtin_convertvector(v, bf16x2)); }
DI float rbf(float f) { return bflo(pk2(f, 0.f) & 0xffffu); }
template <int CTRL, int RMASK>
DI float dppmov(float v) { return __int_as_float(__builtin_amdgcn_update_dpp(0, __float_as_int(v), CTRL, RMASK, 0xf, false)); }
DI float wave_sum(float v) {
  v += dppmov<0x111, 0xf>(v); v += dppmov<0x112, 0xf>(v); v += dppmov<0x114, 0xf>(v); v += dppmov<0x118, 0xf>(v);
  v += dppmov<0x142, 0xa>(v); v += dppmov<0x143, 0xc>(v);
  return __int_as_float(__builtin_amdgcn_readlane(__float_as_int(v), 63));
}
DI float swap32_add(float v) { auto r = __builtin_amdgcn_permlane32_swap(__float_as_uint(v), __float_as_uint(v), false, false); return __uint_as_float(r[0]) + __uint_as_float(r[1]); }
DI float swap16_add(float v) { auto r = __builtin_amdgcn_permlane16_swap(__float_as_uint(v), __float_as_uint(v), false, false); return __uint_as_float(r[0]) + __uint_as_float(r[1]); }
DI f32x2 gelu2(f32x2 z) {
  const float c0 = -2.f * 0.7978845608028654f * 1.4426950408889634f, c1 = c0 * 0.044715f;
  const f32x2 C0 = {c0, c0}, C1 = {c1, c1}, one = {1.f, 1.f};
  f32x2 a = z * __builtin_elementwise_fma(z * z, C1, C0);
  f32x2 e = {__builtin_amdgcn_exp2f(a[0]), __builtin_amdgcn_exp2f(a[1])};
  e = e + one;
  f32x2 r = {__builtin_amdgcn_rcpf(e[0]), __builtin_amdgcn_rcpf(e[1])};
  return z * r;
}
DI u32 gelu_ln2(float a0, float a1, float c0, float c1, float m, float r) {
  const f32x2 M = {-m, -m}, R = {r, r};
  f32x2 z = __builtin_elementwise_fma((f32x2){c0, c1}, M, (f32x2){a0, a1}) * R;
  return pk2v(gelu2(z));
}
DI void glds16(u32 voff, const void* sbase, u32 lds) {
  asm volatile("s_mov_b32 m0, %2\n\ts_nop 0\n\tglobal_load_lds_dwordx4 %0, %1 sc1" :: "v"(voff), "s"(sbase), "s"(lds) : "memory");
}
DI u32 lds_base(char* smem) { return (u32)(uintptr_t)(LAS char*)smem; }
#define WAITV(n) asm volatile("s_waitcnt vmcnt(" #n ")" ::: "memory")
#define WAITV_T(n) asm volatile("s_waitcnt vmcnt(%0)" :: "i"(n) : "memory")
#define SB() __builtin_amdgcn_sched_barrier(0)
#define RAWBAR() do { SB(); __builtin_amdgcn_s_barrier(); SB(); } while (0)
#define BAR() do { SB(); asm volatile("s_waitcnt lgkmcnt(0)" ::: "memory"); __builtin_amdgcn_s_barrier(); asm volatile("" ::: "memory"); SB(); } while (0)
DI bf16x8 tr_frag(char* smem, u32 a0, u32 a1) {
  s16x4 a = __builtin_amdgcn_ds_read_tr16_b64_v4i16((LAS s16x4*)(smem + a0));
  s16x4 b = __builtin_amdgcn_ds_read_tr16_b64_v4i16((LAS s16x4*)(smem + a1));
  return __builtin_bit_cast(bf16x8, __builtin_shufflevector(a, b, 0, 1, 2, 3, 4, 5, 6, 7));
}
DI f32x16 mfma32(bf16x8 a, bf16x8 b, f32x16 c) { return __builtin_amdgcn_mfma_f32_32x32x16_bf16(a, b, c, 0, 0, 0); }
DI f32x4 mfma16(bf16x8 a, bf16x8 b, f32x4 c) { return __builtin_amdgcn_mfma_f32_16x16x32_bf16(a, b, c, 0, 0, 0); }
DI bf16x8 asbf(u32x4 v) { return __builtin_bit_cast(bf16x8, v); }
typedef __amdgpu_buffer_rsrc_t rsrc_t;
DI u32x4 ld16(rsrc_t rs, u32 voff, u32 soff) { return __builtin_amdgcn_raw_buffer_load_b128(rs, voff, soff, 16); }
DI void st16(rsrc_t rs, u32 off, u32x4 v) { __builtin_amdgcn_raw_buffer_store_b128(v, rs, off, 0, 16); }
template <int N, int LO, int HI>
DI void ldw_(u32x4 (&a)[N], rsrc_t rs, u32 base, int lane, int rot) {
#pragma unroll
  for (int i = LO; i < HI; ++i) a[i] = ld16(rs, lane * 16, base + ((i & ~15) + (((i & 15) + rot) & 15)) * 1024);
  SB();
}
#define LDW(N, LO, HI, arr, base) ldw_<N, LO, HI>(arr, rs, base, lane, rot)

DI u32 img128_srcoff(int lane) { return ((((lane >> 2) & 1) ^ ((lane >> 4) & 1)) * 64) + (lane & 3) * 16; }
DI u32 img128_traddr(int lane, int ct) {
  int h = lane >> 5, blk = (lane >> 4) & 1, q = (lane & 15) >> 2, p = lane & 3;
  return (4 * h + q) * 128 + ((ct ^ ((q >> 1) & 1)) * 64) + blk * 32 + p * 8;
}

struct KTab { u16 v[KR_LEN + 64]; };
constexpr int bf16_round_pos(int t) {
  if (t < 256) return t;
  int sh = t < 512 ? 1 : t < 1024 ? 2 : 3;
  int q = t >> sh, r = t & ((1 << sh) - 1), half = 1 << (sh - 1);
  if (r > half || (r == half && (q & 1))) ++q;
  return q << sh;
}
constexpr KTab make_ktab() {
  KTab t{};
  for (int i = 0; i < KR_LEN + 64; ++i) {
    int tau = KR_OFF - i; int idx = 1024;
    if (tau >= 0 && tau < L) {
      int p = bf16_round_pos(tau); int m = 0, col = 0;
      if (p < 256) { m = p & 7; col = p >> 3; }
      else if (p < 512) { m = 8 + ((p & 7) >> 1); col = (p - 256) >> 3; }
      else if (p < 1024) { m = 12 + 2 * ((p - 512) >> 8) + ((p >> 2) & 1); col = (p & 255) >> 3; }
      else { m = 16 + ((p - 1024) >> 8); col = (p & 255) >> 3; }
      idx = m * 32 + col;
    }
    t.v[i] = (u16)idx;
  }
  return t;
}
__device__ const KTab g_ktab = make_ktab();

struct cmx { f32x2 a, b; };
DI cmx cmx_of(f32x2 v) { return {v, (f32x2){-v[1], v[0]}}; }
DI f32x2 cmul2(f32x2 e, const cmx& m) { return __builtin_elementwise_fma((f32x2){e[1], e[1]}, m.b, (f32x2){e[0], e[0]} * m.a); }
DI f32x2 cexp_p(float ar, float ai_rev, float p) {
  float e = __builtin_amdgcn_exp2f(ar * p * 1.4426950408889634f);
  float rv = __builtin_amdgcn_fractf(ai_rev * p);
  return (f32x2){e * __builtin_amdgcn_cosf(rv), e * __builtin_amdgcn_sinf(rv)};
}

constexpr int KC_STR = 4416, KC_CH = 8 * KC_STR;
constexpr int U_QS = 1568, U_CH = 4 * U_QS;
constexpr int LD_U = 2 * KC_CH;
constexpr int LD_X = LD_U + 2 * U_CH;
constexpr int LD_KV = 96 * 1024;
constexpr int LD_LN = 128 * 1024;
constexpr int LD_KS = 144 * 1024;
constexpr int LD_EX = 128 * 1024;
constexpr int LD_SM = LD_EX + 16384;
constexpr int LD_CS1 = LD_SM + 1024, LD_CS3 = LD_CS1 + 1024;
constexpr int LD_PF = 144 * 1024 + 2 * 4224 + 256;
constexpr int LDS_BYTES = LD_PF + 1024;
static_assert(LD_X + 2 * U_CH <= LD_KV && LD_LN + 16384 <= LD_KS && LD_CS3 + 1024 <= LD_PF && (LD_PF & 15) == 0 && LDS_BYTES <= 160 * 1024, "lds");

constexpr unsigned SPIN_CAP = 1u << 18;
constexpr u64 TAG_INC = 0x9E3779B97F4A7C15ull;

DI void publish(char* ws, int phase, int id, u64 tag) {
  RAWBAR();
  if (threadIdx.x == 0) __hip_atomic_store((gu64*)(ws + WS_FL + (phase * 256 + id) * 128), tag, RLX_AGENT);
}
DI void wait_flags(char* ws, int phase, int idx, int w, u64 tag) {
  if (w == 0) {
    for (unsigned spins = 0; spins < SPIN_CAP; ++spins) {
      u64 f = tag;
      if (idx >= 0) f = __hip_atomic_load((gu64*)(ws + WS_FL + (phase * 256 + idx) * 128), RLX_AGENT);
      if (__all(f == tag)) break;
    }
  }
  RAWBAR();
}
DI void wait_all(char* ws, int phase, int w, int lane, u64 tag) {
  if (w == 0) {
    gu64* fp = (gu64*)(ws + WS_FL + (phase * 256 + lane) * 128);
    for (unsigned spins = 0; spins < SPIN_CAP; ++spins) {
      u64 f0 = __hip_atomic_load(fp, RLX_AGENT), f1 = __hip_atomic_load(fp + 64 * 16, RLX_AGENT);
      u64 f2 = __hip_atomic_load(fp + 128 * 16, RLX_AGENT), f3 = __hip_atomic_load(fp + 192 * 16, RLX_AGENT);
      if (__all(f0 == tag && f1 == tag && f2 == tag && f3 == tag)) break;
    }
  }
  RAWBAR();
}

#define NW3A 8

__global__ void __launch_bounds__(256, 1) kmain(const u16* __restrict__ x, const u16* __restrict__ logA, const u16* __restrict__ Aim,
                                                const u16* __restrict__ Cr, const u16* __restrict__ Ci, const u16* __restrict__ logdt,
                                                const u16* __restrict__ dskip, const u16* __restrict__ w1g, const u16* __restrict__ w2g,
                                                const u16* __restrict__ w3g, const u16* __restrict__ w4g, char* ws, u16* out) {
  extern __shared__ __attribute__((aligned(16))) char smem[];
  const int tid = threadIdx.x, lane = tid & 63, w = __builtin_amdgcn_readfirstlane(tid >> 6);
  const int b = blockIdx.x, xcd = b & 7, idx = b >> 3;
  const u32 lb = lds_base(smem);
  const rsrc_t rs = __builtin_amdgcn_make_buffer_rsrc(ws, 0, WS_SPAN, 0x00020000);
  const int h = lane >> 5;
  u32x4 xv[2];
#pragma unroll
  for (int r = 0; r < 2; ++r) xv[r] = *(const GAS u32x4*)(x + (size_t)(8 * b + 2 * w + r) * D + lane * 8);
  const u64 seed_v = __hip_atomic_load((gu64*)(ws + WS_SEED), RLX_AGENT);
  SB();
  {
    u64 pc; u32 clen;
    asm volatile("s_getpc_b64 %0\nkm_code_begin:\n\ts_mov_b32 %1, km_code_end-km_code_begin" : "=s"(pc), "=s"(clen));
    const u32 chunk = (idx * 4 + w) * 1024;
    if (chunk < clen) {
      const u32 vo = min(chunk + lane * 16, clen - 16) & ~15u;
      asm volatile("s_mov_b32 m0, %2\n\ts_nop 0\n\tglobal_load_lds_dwordx4 %0, %1" :: "v"(vo), "s"(pc & ~15ull), "s"(lb + LD_PF) : "memory");
    }
    if (w == 3) {
      const u32 vo = (lane & 15) << 21;
      asm volatile("s_mov_b32 m0, %2\n\ts_nop 0\n\tglobal_load_lds_dword %0, %1" :: "v"(vo), "s"(ws), "s"(lb + LD_PF) : "memory");
    }
  }
  u64 tag;

  const int panel = xcd * 4 + (idx & 3), ct = idx >> 2, me = panel * 8 + ct;
  const int kh = w >> 1, nh = w & 1, tto = kh ^ nh;
  const u32 w1p = WS_WT1 + ((ct * 2 + nh) * 2 + kh) * 16384, w2p = WS_WT2 + ((ct * 2 + nh) * 2 + kh) * 16384;
  const u32 w3p = WS_WT3 + (ct * 4 + w) * 65536, w4p = WS_WT4 + ((ct * 2 + nh) * 2 + kh) * 56 * 1024;
  u32x4 w1[16], w2[16], w3[64];
  const int rot = (ct & 3) * 4;
#define ROT(s) (((s) + rot) & 15)
  u32x4 w4[56];
#define JL(i) ((i) < 32 ? 14 * (((((i) >> 2) + ct) & 7) >> 1) + 6 * ((((i) >> 2) + ct) & 1) + ((i) & 1) + 3 * (((i) >> 1) & 1) \
                        : 14 * (((((i) - 32) / 6) + ct) & 3) + ((((i) - 32) % 6) < 4 ? (((i) - 32) % 6) * 3 + 2 : (((i) - 32) % 6) + 8))
#define LDW4(LO, HI) do { _Pragma("unroll") for (int i_ = (LO); i_ < (HI); ++i_) w4[i_] = ld16(rs, lane * 16, w4p + JL(i_) * 1024); SB(); } while (0)

  const int c0 = 2 * (xcd * 32 + idx);
  const int chl = w >> 1, ih = w & 1, ch = c0 + chl;
  {
    const int n = 64 * ih + lane;
    const u32 p_logA = logA[ch * NS + n], p_Aim = Aim[ch * NS + n], p_Cr = Cr[ch * NS + n], p_Ci = Ci[ch * NS + n], p_dt = logdt[ch];
    u32x4 ktab[3];
#pragma unroll
    for (int it = 0; it < 3; ++it) { int U = 63 * (it * 4 + w) + lane; int u8 = U >= KR_LEN / 8 ? U - KR_LEN / 8 : U; if (u8 >= KR_LEN / 8) u8 = 0; ktab[it] = *(const GAS u32x4*)(g_ktab.v + u8 * 8); }
    if (tid < 2) *(float*)(smem + LD_KS + tid * 4224 + 4096) = 0.f;
    int u = w * 256 + b; const bool has_unit = u < 576;
    const u16* W = w1g; int N = D, ku = 0, nu = 0, kind = 0;
    if (u < 64) { W = w1g; ku = u >> 3; nu = u & 7; kind = 0; }
    else if (u < 128) { u -= 64; W = w2g; ku = u >> 3; nu = u & 7; kind = 1; }
    else if (u < 352) { u -= 128; W = w4g; ku = u >> 3; nu = u & 7; kind = 2; }
    else if (u < 576) { u -= 352; W = w3g; N = F; ku = u / 28; nu = u % 28; kind = 3; }
    u32x4 wv[8];
    if (has_unit) {
      const u16* src = W + (size_t)(ku * 64 + (lane >> 3)) * N + nu * 64 + (lane & 7) * 8;
#pragma unroll
      for (int i = 0; i < 8; ++i) wv[i] = *(const GAS u32x4*)(src + (size_t)i * 8 * N);
    }
    SB();
    const float l2e = 1.4426950408889634f;
    const float dt = rbf(__builtin_amdgcn_exp2f(bf2f(p_dt) * l2e));
    const float ar = -rbf(__builtin_amdgcn_exp2f(bf2f(p_logA) * l2e)) * dt;
    const float ai = bf2f(p_Aim) * dt * 0.15915494309189535f;
    const f32x2 cc = {2.f * bf2f(p_Cr) * dt, 2.f * bf2f(p_Ci) * dt};
    asm volatile("" :: "v"(ar), "v"(ai), "v"(cc[0]), "v"(cc[1]), "v"(ktab[0][0]), "v"(ktab[1][0]), "v"(ktab[2][0]));
    tag = (((u64)(u32)__builtin_amdgcn_readfirstlane((u32)(seed_v >> 32)) << 32) | (u32)__builtin_amdgcn_readfirstlane((u32)seed_v)) + TAG_INC;
    {
      float uo[2][8], v[2][8], sm[2], sq[2];
#pragma unroll
      for (int r = 0; r < 2; ++r) {
#pragma unroll
        for (int j = 0; j < 4; ++j) { v[r][2 * j] = bflo(xv[r][j]); v[r][2 * j + 1] = bfhi(xv[r][j]); }
        sm[r] = ((v[r][0] + v[r][1]) + (v[r][2] + v[r][3])) + ((v[r][4] + v[r][5]) + (v[r][6] + v[r][7]));
      }
#pragma unroll
      for (int r = 0; r < 2; ++r) sm[r] = wave_sum(sm[r]) * (1.f / D);
#pragma unroll
      for (int r = 0; r < 2; ++r) {
        sq[r] = 0.f;
#pragma unroll
        for (int j = 0; j < 8; ++j) { v[r][j] -= sm[r]; sq[r] = __builtin_fmaf(v[r][j], v[r][j], sq[r]); }
      }
#pragma unroll
      for (int r = 0; r < 2; ++r) {
        const float rsb = __builtin_amdgcn_rsqf(wave_sum(sq[r]) * (1.f / D) + 1e-5f);
#pragma unroll
        for (int j = 0; j < 8; ++j) uo[r][j] = v[r][j] * rsb;
      }
      const int sw = lane & 7;
#pragma unroll
      for (int j = 0; j < 8; ++j) {
        char* row = smem + LD_LN + (lane * 8 + j) * 32;
        const u32 x0 = xv[0][j >> 1], x1 = xv[1][j >> 1];
        *(u32*)(row + ((w ^ sw) * 4)) = pk2(uo[0][j], uo[1][j]);
        *(u32*)(row + (((4 + w) ^ sw) * 4)) = (j & 1) ? ((x0 >> 16) | (x1 & 0xffff0000u)) : ((x0 & 0xffffu) | (x1 << 16));
      }
    }
    BAR();
#pragma unroll
    for (int it = 0; it < 4; ++it) {
      const int unit = it * 256 + tid, cc = unit >> 1, half = unit & 1, s = (cc >> 3) & 7;
      u32x4 v = *(const u32x4*)(smem + LD_LN + cc * 32 + (((4 * half) ^ (s & 4)) * 4));
      if (s & 1) v = (u32x4){v[1], v[0], v[3], v[2]};
      if (s & 2) v = (u32x4){v[2], v[3], v[0], v[1]};
      st16(rs, WS_UX + b * 16384 + unit * 16, v);
    }
    {
      const f32x2 c = cc;
      char* rowB = smem + w * (53 * 272) + lane * 4;
      char* rowA = rowB + 32 * 272;
      const cmx W1 = cmx_of(cexp_p(ar, ai, 1.f)), W8 = cmx_of(cexp_p(ar, ai, 8.f)), E256 = cmx_of(cexp_p(ar, ai, 256.f));
      const f32x2 e64 = cexp_p(ar, ai, 64.f), e128 = cexp_p(ar, ai, 128.f), e192 = cexp_p(ar, ai, 192.f);
      const f32x2 cj = {1.f, -1.f};
      f32x2 e0 = {1.f, 0.f}, e1 = e64, e2 = e128, e3 = e192, ea = c;
#pragma unroll
      for (int col = 0; col < 8; ++col) {
        *(u32*)(rowB + col * 272) = pk2v(e0); *(u32*)(rowB + (8 + col) * 272) = pk2v(e1);
        *(u32*)(rowB + (16 + col) * 272) = pk2v(e2); *(u32*)(rowB + (24 + col) * 272) = pk2v(e3);
        *(u32*)(rowA + col * 272) = pk2v(ea * cj);
        e0 = cmul2(e0, W8); e1 = cmul2(e1, W8); e2 = cmul2(e2, W8); e3 = cmul2(e3, W8); ea = cmul2(ea, W1);
      }
      const f32x2 w1v = W1.a, w2v = cmul2(w1v, W1); const cmx W2 = cmx_of(w2v); const cmx W4 = cmx_of(cmul2(w2v, W2));
      const f32x2 c256 = cmul2(c, E256), c512 = cmul2(c256, E256), c768 = cmul2(c512, E256), c1024 = cmul2(c768, E256);
      f32x2 eb = c256, ec = c1024;
#pragma unroll
      for (int m = 0; m < 4; ++m) {
        *(u32*)(rowA + (8 + m) * 272) = pk2v(eb * cj); *(u32*)(rowA + (16 + m) * 272) = pk2v(ec * cj);
        eb = cmul2(eb, W2); ec = cmul2(ec, E256);
      }
      *(u32*)(rowA + 20 * 272) = pk2v(ec * cj);
      *(u32*)(rowA + 12 * 272) = pk2v(c512 * cj); *(u32*)(rowA + 13 * 272) = pk2v(cmul2(c512, W4) * cj);
      *(u32*)(rowA + 14 * 272) = pk2v(c768 * cj); *(u32*)(rowA + 15 * 272) = pk2v(cmul2(c768, W4) * cj);
    }
    WAITV(0);
    publish(ws, 0, b, tag);
    if (has_unit) {
      char* img = smem + LD_KV + w * 8192; const u32 imgo = LD_KV + w * 8192;
      const int pc = lane & 7;
#pragma unroll
      for (int i = 0; i < 8; ++i) {
        const int r = i * 8 + (lane >> 3);
        const int pp = (kind == 3) ? ((((pc >> 1) ^ ((r >> 1) & 3)) << 1) | (pc & 1)) : ((((pc >> 2) ^ ((r >> 1) & 1)) << 2) | (pc & 3));
        *(u32x4*)(img + r * 128 + pp * 16) = wv[i];
      }
      if (kind != 3) {
        const int ksh = kind == 2 ? 56 : 16;
        const u32 dst = kind == 0 ? WS_WT1 : kind == 1 ? WS_WT2 : WS_WT4;
#pragma unroll
        for (int hn = 0; hn < 2; ++hn) {
          const u32 a = imgo + img128_traddr(lane, hn);
#pragma unroll
          for (int s4 = 0; s4 < 4; ++s4) {
            bf16x8 f = tr_frag(smem, a + s4 * 2048, a + s4 * 2048 + 1024);
            const int s = ku * 4 + s4; const int k2 = s >= ksh; const int sl = s - k2 * ksh;
            st16(rs, dst + (((nu * 2 + hn) * 2 + k2) * ksh + sl) * 1024 + lane * 16, __builtin_bit_cast(u32x4, f));
          }
        }
      } else {
        const int g = lane >> 4, q = (lane & 15) >> 2, p = lane & 3;
#pragma unroll
        for (int c16 = 0; c16 < 4; ++c16) {
          const int j = nu * 4 + c16; const int tc = j / 14, r14 = j % 14;
#pragma unroll
          for (int sp = 0; sp < 2; ++sp) {
            const int r0 = 32 * sp + 16 * (g >> 1) + 4 * (g & 1) + q, r1 = r0 + 8;
            bf16x8 f = tr_frag(smem, imgo + r0 * 128 + ((c16 ^ ((r0 >> 1) & 3)) * 32) + p * 8, imgo + r1 * 128 + ((c16 ^ ((r1 >> 1) & 3)) * 32) + p * 8);
            const u32 dst = WS_WT3 + tc * 4 * 65536 + (ku * 2 + sp) * 1024 + lane * 16;
            if (r14 < 12) st16(rs, dst + (r14 / 3) * 65536 + (r14 % 3) * 16384, __builtin_bit_cast(u32x4, f));
            else { const int w0 = 2 * (r14 - 12); st16(rs, dst + w0 * 65536 + 3 * 16384, __builtin_bit_cast(u32x4, f)); st16(rs, dst + (w0 + 1) * 65536 + 3 * 16384, __builtin_bit_cast(u32x4, f)); }
          }
        }
      }
    }
    if (b >= 252) {
      const u32 dst = WS_H1 + ((b & 2) ? 65u * 32 * 1024 : 0u) + (b & 1) * 16384 + tid * 16; const u32x4 z = {0, 0, 0, 0};
#pragma unroll
      for (int i = 0; i < 4; ++i) st16(rs, dst + i * 4096, z);
    }
    f32x16 kacc = {};
    {
      const int m = lane & 31;
      bf16x8 af[8], bf[8];
#pragma unroll
      for (int s = 0; s < 8; ++s) {
        af[s] = *(const bf16x8*)(smem + w * (53 * 272) + (32 + m) * 272 + 32 * s + 16 * h);
        bf[s] = *(const bf16x8*)(smem + w * (53 * 272) + m * 272 + 32 * s + 16 * h);
      }
      SB();
#pragma unroll
      for (int s = 0; s < 8; ++s) kacc = mfma32(af[s], bf[s], kacc);
    }
    BAR();
    {
      float* kv = (float*)(smem + LD_KV + w * 4096);
      const int col = lane & 31;
#pragma unroll
      for (int r = 0; r < 16; ++r) kv[((r & 3) + 8 * (r >> 2) + 4 * h) * 32 + col] = kacc[r];
    }
    BAR();
#pragma unroll
    for (int it = 0; it < 2; ++it) {
      const int i4 = it * 256 + tid, c2 = i4 >> 8, e4 = i4 & 255;
      const f32x4 a = *(const f32x4*)(smem + LD_KV + (2 * c2) * 4096 + e4 * 16), bq = *(const f32x4*)(smem + LD_KV + (2 * c2 + 1) * 4096 + e4 * 16);
      *(f32x4*)(smem + LD_KS + c2 * 4224 + e4 * 16) = a + bq;
    }
    { const int c2 = tid >> 7, q = (tid >> 5) & 3, cc = tid & 31; *(u32x4*)(smem + LD_U + c2 * U_CH + q * U_QS + cc * 16) = (u32x4){0, 0, 0, 0}; }
    asm volatile("s_waitcnt vmcnt(0) lgkmcnt(0)" ::: "memory");
    publish(ws, 6, b, tag);
#pragma unroll
    for (int it = 0; it < 3; ++it) {
      const int U = 63 * (it * 4 + w) + lane;
      const int c2 = U >= KR_LEN / 8, u8 = U - c2 * (KR_LEN / 8);
      const float* kv = (const float*)(smem + LD_KS + (c2 & 1) * 4224);
      u32 d[8];
#pragma unroll
      for (int j = 0; j < 4; ++j) d[j] = pk2(kv[ktab[it][j] & 0xffffu], kv[ktab[it][j] >> 16]);
#pragma unroll
      for (int j = 0; j < 4; ++j) d[4 + j] = __builtin_amdgcn_update_dpp(0, d[j], 0x130, 0xf, 0xf, false);
      if (lane < 63 && U < 2 * (KR_LEN / 8)) {
        u32 a[7];
#pragma unroll
        for (int k = 0; k < 7; ++k) a[k] = __builtin_amdgcn_alignbyte(d[k + 1], d[k], 2);
        char* dst = smem + c2 * KC_CH + u8 * 16;
#pragma unroll
        for (int k = 0; k < 4; ++k) {
          *(u32x4*)(dst + (7 - 2 * k) * KC_STR) = (u32x4){d[k], d[k + 1], d[k + 2], d[k + 3]};
          *(u32x4*)(dst + (6 - 2 * k) * KC_STR) = (u32x4){a[k], a[k + 1], a[k + 2], a[k + 3]};
        }
      }
    }
    }
  wait_all(ws, 0, w, lane, tag);
  if (b == 0 && tid == 0) __hip_atomic_store((gu64*)(ws + WS_SEED), tag, RLX_AGENT);
  {
    const float dsk = bf2f(dskip[ch]);
    {
      u32x4 gv[4];
#pragma unroll
      for (int i = 0; i < 4; ++i) gv[i] = ld16(rs, (64 * w + 16 * i + (lane >> 2)) * 16384 + (lane & 3) * 16, WS_UX + c0 * 32);
      gu64* fp = (gu64*)(ws + WS_FL + (6 * 256 + lane) * 128);
      u64 f0 = tag, f1 = tag, f2 = tag, f3 = tag;
      if (w == 0) { f0 = __hip_atomic_load(fp, RLX_AGENT); f1 = __hip_atomic_load(fp + 64 * 16, RLX_AGENT); f2 = __hip_atomic_load(fp + 128 * 16, RLX_AGENT); f3 = __hip_atomic_load(fp + 192 * 16, RLX_AGENT); }
#pragma unroll
      for (int i = 0; i < 4; ++i) {
        const int tb = 64 * w + 16 * i + (lane >> 2), pc = lane & 3;
        *(u32x4*)(smem + ((pc & 1) ? LD_X : LD_U) + (pc >> 1) * U_CH + (tb & 3) * U_QS + (32 + (tb >> 2)) * 16) = gv[i];
      }
      if (w == 0) {
        for (unsigned spins = 0; spins < SPIN_CAP; ++spins) {
          if (__all(f0 == tag && f1 == tag && f2 == tag && f3 == tag)) break;
          f0 = __hip_atomic_load(fp, RLX_AGENT); f1 = __hip_atomic_load(fp + 64 * 16, RLX_AGENT); f2 = __hip_atomic_load(fp + 128 * 16, RLX_AGENT); f3 = __hip_atomic_load(fp + 192 * 16, RLX_AGENT);
        }
      }
    }
    BAR();
    const int n = lane & 31;
    f32x16 acc0 = {}, acc1 = {};
    {
      const u32 abase = chl * KC_CH + (n & 7) * KC_STR + 16 * (257 + h - (n >> 3)) - 32 * (32 * ih - 1) - 32 * 95;
      const u32 ubase = LD_U + chl * U_CH + h * U_QS + (32 + n - 16 * ih) * 16 - 16 * 16;
      bf16x8 A1[3], A2[3], b0[3], b1[3];
      auto ld = [&](int ii) {
        const int k = ii % 3;
        A1[k] = *(const bf16x8*)(smem + abase + 32 * (95 - ii));
        A2[k] = *(const bf16x8*)(smem + abase + 32 * (95 - 64 - ii));
        const u32 pb = (ii & 1) ? (ubase + (16 - (ii - 1) / 2) * 16) : (ubase + 2 * U_QS + (16 - ii / 2) * 16);
        b0[k] = *(const bf16x8*)(smem + pb); b1[k] = *(const bf16x8*)(smem + pb + 32 * 16);
      };
      ld(0); ld(1);
#define CONV_STEP(ii) { if ((ii) + 2 < 32) ld((ii) + 2); SB(); \
        acc0 = mfma32(A1[(ii) % 3], b0[(ii) % 3], acc0); acc1 = mfma32(A1[(ii) % 3], b1[(ii) % 3], acc1); acc1 = mfma32(A2[(ii) % 3], b0[(ii) % 3], acc1); SB(); \
        if ((ii) < 16) LDW(16, (ii) < 16 ? (ii) : 0, (ii) < 16 ? (ii) + 1 : 0, w1, w1p); else LDW(16, (ii) >= 16 ? (ii) - 16 : 0, (ii) >= 16 ? (ii) - 15 : 0, w2, w2p); }
      CONV_STEP(0) CONV_STEP(1) CONV_STEP(2) CONV_STEP(3) CONV_STEP(4) CONV_STEP(5) CONV_STEP(6) CONV_STEP(7)
      CONV_STEP(8) CONV_STEP(9) CONV_STEP(10) CONV_STEP(11) CONV_STEP(12) CONV_STEP(13) CONV_STEP(14) CONV_STEP(15)
      CONV_STEP(16) CONV_STEP(17) CONV_STEP(18) CONV_STEP(19) CONV_STEP(20) CONV_STEP(21) CONV_STEP(22) CONV_STEP(23)
      CONV_STEP(24) CONV_STEP(25) CONV_STEP(26) CONV_STEP(27) CONV_STEP(28) CONV_STEP(29) CONV_STEP(30) CONV_STEP(31)
    }
    BAR();
    {
      f32x16 snd = ih ? acc0 : acc1;
      char* dst = smem + w * 4096 + lane * 16;
#pragma unroll
      for (int q = 0; q < 4; ++q) *(f32x4*)(dst + q * 1024) = (f32x4){snd[4 * q], snd[4 * q + 1], snd[4 * q + 2], snd[4 * q + 3]};
    }
    BAR();
    {
      f32x16 y = ih ? acc1 : acc0;
      const char* src = smem + (w ^ 1) * 4096 + lane * 16;
      u32x2 o[4];
#pragma unroll
      for (int q = 0; q < 4; ++q) {
        const f32x4 p = *(const f32x4*)(src + q * 1024);
        const u32 io = chl * U_CH + q * U_QS + (32 + 32 * ih + n) * 16 + 8 * h;
        const u32x2 uu = *(const u32x2*)(smem + LD_U + io), xx = *(const u32x2*)(smem + LD_X + io);
#pragma unroll
        for (int e = 0; e < 2; ++e) {
          const f32x2 yy = (f32x2){y[4 * q + 2 * e], y[4 * q + 2 * e + 1]} + (f32x2){p[2 * e], p[2 * e + 1]} + (f32x2){bflo(xx[e]), bfhi(xx[e])};
          o[q][e] = pk2v(__builtin_elementwise_fma((f32x2){bflo(uu[e]), bfhi(uu[e])}, (f32x2){dsk, dsk}, yy));
        }
      }
      const u32 hp = WS_HP + (16 * ih + (n >> 1)) * 65536 + ch * 128 + (32 * (n & 1) + 8 * h) * 2;
#pragma unroll
      for (int pr = 0; pr < 2; ++pr) {
        auto r0 = __builtin_amdgcn_permlane32_swap(o[2 * pr][0], o[2 * pr + 1][0], false, false);
        auto r1 = __builtin_amdgcn_permlane32_swap(o[2 * pr][1], o[2 * pr + 1][1], false, false);
        st16(rs, hp + pr * 32, (u32x4){r0[0], r1[0], r0[1], r1[1]});
      }
    }
    SB();
    LDW(64, 0, NW3A, w3, w3p);
    {
      const bf16x8 onesv = {(__bf16)1.f, (__bf16)1.f, (__bf16)1.f, (__bf16)1.f, (__bf16)1.f, (__bf16)1.f, (__bf16)1.f, (__bf16)1.f};
      f32x16 cacc = {};
#pragma unroll
      for (int s = 0; s < 16; ++s) cacc = mfma32(asbf(w1[s]), onesv, cacc);
      if ((lane & 31) == 0) {
        float* dst = (float*)(smem + LD_CS1) + (kh * 2 + nh) * 32 + 4 * h;
#pragma unroll
        for (int q = 0; q < 4; ++q) *(f32x4*)(dst + 8 * q) = (f32x4){cacc[4 * q], cacc[4 * q + 1], cacc[4 * q + 2], cacc[4 * q + 3]};
      }
    }
    SB();
    WAITV_T(NW3A);
    publish(ws, 1, b, tag);
    if (w != 0) LDW(64, NW3A, 40, w3, w3p); else LDW(64, NW3A, 20, w3, w3p);
    wait_all(ws, 1, w, lane, tag);
  }

  const int T = panel * 2 + tto; const int s_out = ct * 4 + nh * 2;
  char* exw = smem + LD_EX + w * 4096 + lane * 16;
  const char* exr = smem + LD_EX + (w ^ 2) * 4096 + lane * 16;
  u32x4 hres[2], xb[2], x1r[2];
  {
    {
      const int half = w >> 1, sub = w & 1;
      const char* sl = ws + WS_HP + (size_t)panel * 65536 + (half * 256 + sub * 32) * 128;
      const u32 vo = (lane >> 3) * 128 + img128_srcoff(lane);
#pragma unroll
      for (int c = 0; c < 3; ++c)
#pragma unroll
        for (int i = 0; i < 4; ++i) glds16(vo, sl + (ROT(4 * c) * 16 + i * 8) * 128, lb + (half * 256 + sub * 32 + c * 64 + i * 8) * 128);
      SB();
      f32x16 acc0 = {}, acc1 = {};
      float sa0 = 0.f, sa1 = 0.f, sq0 = 0.f, sq1 = 0.f;
      const u32 aB0 = kh * 256 * 128 + img128_traddr(lane, tto), aB1 = kh * 256 * 128 + img128_traddr(lane, tto ^ 1);
      const bf16x2 ones = {(__bf16)1.0f, (__bf16)1.0f};
      bf16x8 b0[2], b1[2];
      WAITV(8); RAWBAR();
      b0[0] = tr_frag(smem, aB0, aB0 + 1024); b1[0] = tr_frag(smem, aB1, aB1 + 1024);
#pragma unroll
      for (int s = 0; s < 16; ++s) {
        if (s + 1 < 16) {
          if (s + 1 == 4) { WAITV(7); RAWBAR(); } else if (s + 1 == 8) { WAITV(4); RAWBAR(); } else if (s + 1 == 12) { WAITV(0); RAWBAR(); }
          b0[(s + 1) & 1] = tr_frag(smem, aB0 + (s + 1) * 2048, aB0 + (s + 1) * 2048 + 1024);
          b1[(s + 1) & 1] = tr_frag(smem, aB1 + (s + 1) * 2048, aB1 + (s + 1) * 2048 + 1024);
        }
        SB();
        acc0 = mfma32(asbf(w1[s]), b0[s & 1], acc0); acc1 = mfma32(asbf(w1[s]), b1[s & 1], acc1);
        SB();
        if (s < 4) glds16(vo, sl + (ROT(12) * 16 + s * 8) * 128, lb + (half * 256 + sub * 32 + 3 * 64 + s * 8) * 128);
        { const bf16x8 st = b0[s & 1];
          const bf16x2 p0 = {st[0], st[1]}, p1 = {st[2], st[3]}, p2 = {st[4], st[5]}, p3 = {st[6], st[7]};
          sa0 = __builtin_amdgcn_fdot2_f32_bf16(p0, ones, sa0, false); sq0 = __builtin_amdgcn_fdot2_f32_bf16(p0, p0, sq0, false);
          sa1 = __builtin_amdgcn_fdot2_f32_bf16(p1, ones, sa1, false); sq1 = __builtin_amdgcn_fdot2_f32_bf16(p1, p1, sq1, false);
          sa0 = __builtin_amdgcn_fdot2_f32_bf16(p2, ones, sa0, false); sq0 = __builtin_amdgcn_fdot2_f32_bf16(p2, p2, sq0, false);
          sa1 = __builtin_amdgcn_fdot2_f32_bf16(p3, ones, sa1, false); sq1 = __builtin_amdgcn_fdot2_f32_bf16(p3, p3, sq1, false); }
        if (s >= 12 && w == 0) { if (s == 12) LDW(64, 20, 25, w3, w3p); else if (s == 13) LDW(64, 25, 30, w3, w3p); else if (s == 14) LDW(64, 30, 35, w3, w3p); else LDW(64, 35, 40, w3, w3p); }
        SB();
      }
      float ssum = sa0 + sa1, ssq = sq0 + sq1;
#pragma unroll
      for (int q = 0; q < 4; ++q) *(f32x4*)(exw + q * 1024) = (f32x4){acc1[4 * q], acc1[4 * q + 1], acc1[4 * q + 2], acc1[4 * q + 3]};
      ssum = swap32_add(ssum); ssq = swap32_add(ssq);
      if (lane < 32) *(f32x2*)(smem + LD_SM + ((kh * 2 + tto) * 32 + lane) * 8) = (f32x2){ssum, ssq};
      BAR();
      f32x16 z = acc0;
#pragma unroll
      for (int q = 0; q < 4; ++q) { f32x4 p = *(const f32x4*)(exr + q * 1024);
#pragma unroll
        for (int j = 0; j < 4; ++j) z[4 * q + j] += p[j]; }
      f32x4 cs[4];
#pragma unroll
      for (int q = 0; q < 4; ++q) cs[q] = *(const f32x4*)(smem + LD_CS1 + ((0 * 2 + nh) * 32 + 8 * q + 4 * h) * 4) + *(const f32x4*)(smem + LD_CS1 + ((1 * 2 + nh) * 32 + 8 * q + 4 * h) * 4);
      f32x2 s0 = *(const f32x2*)(smem + LD_SM + ((0 * 2 + tto) * 32 + (lane & 31)) * 8);
      f32x2 s1 = *(const f32x2*)(smem + LD_SM + ((1 * 2 + tto) * 32 + (lane & 31)) * 8);
      const float mean = (s0[0] + s1[0]) * (1.f / D);
      const float var = fmaxf((s0[1] + s1[1]) * (1.f / D) - mean * mean, 0.f);
      const float rs1 = __builtin_amdgcn_rsqf(var + 1e-5f);
      const float var2 = var * rs1 * rs1;
      const float rsc = rs1 * __builtin_amdgcn_rsqf(var2 + 1e-5f);
      const u32 ah = img128_traddr(lane, tto);
      const f32x2 R1 = {rs1, rs1}, MR = {-mean * rs1, -mean * rs1};
#pragma unroll
      for (int sp = 0; sp < 2; ++sp) {
        const int s = s_out + sp;
        const u32 apos = ah + ((s & 16) + (((s & 15) - rot) & 15)) * 2048;
        bf16x8 hf = tr_frag(smem, apos, apos + 1024);
        u32x4 hv = __builtin_bit_cast(u32x4, hf);
        u32x4 o;
#pragma unroll
        for (int j = 0; j < 4; ++j) {
          int r = 8 * sp + 2 * j;
          o[j] = gelu_ln2(z[r], z[r + 1], cs[r >> 2][r & 3], cs[(r + 1) >> 2][(r + 1) & 3], mean, rsc);
          xb[sp][j] = pk2v(__builtin_elementwise_fma((f32x2){bflo(hv[j]), bfhi(hv[j])}, R1, MR));
        }
        hres[sp] = hv;
        st16(rs, WS_H1 + 32 * 1024 + (T * 32 + s) * 1024 + lane * 16, o);
      }
    }
    SB();
    LDW(64, 40, 46, w3, w3p);
    WAITV(6);
    publish(ws, 2, me, tag);
    if (w != 0) LDW(64, 46, 64, w3, w3p); else LDW(64, 46, 52, w3, w3p);
  }
  {
    {
      int ix = -1;
      if (lane < 8) ix = panel * 8 + lane;
      else if (lane < 12) { if (panel > 0) ix = (panel - 1) * 8 + ((lane & 1) | ((lane & 2) << 1)); }
      else if (lane < 16) { if (panel < 31) ix = (panel + 1) * 8 + 2 + ((lane & 1) | ((lane & 2) << 1)); }
      wait_flags(ws, 2, ix, w, tag);
    }
    f32x16 c0a = {}, c1a = {};
    {
      const int half = w >> 1, tt = w & 1;
      u32 vo[2];
#pragma unroll
      for (int gg = 0; gg < 2; ++gg) {
        int sh = half ? (gg ? 2 : -2) : (gg ? 1 : -1);
        int tok = tt * 32 + (lane & 31) + sh + 32;
        vo[gg] = (tok >> 5) * 32 * 1024 + ((tok & 31) + 32 * (lane >> 5)) * 16;
      }
      const char* sl = ws + WS_H1 + (size_t)(panel * 2) * 32 * 1024 + (size_t)half * 16 * 1024;
      u32 voc[4];
#pragma unroll
      for (int c = 0; c < 4; ++c) voc[c] = (ROT(4 * c) >> 3) ? vo[1] : vo[0];
#pragma unroll
      for (int s = 0; s < 12; ++s) glds16(voc[s >> 2], sl + ROT(s) * 1024, lb + ((half * 16 + s) * 2 + tt) * 1024);
      SB();
      const u32 aB = kh * 32 * 1024 + lane * 16 + tto * 1024, aBo = aB ^ 1024;
      bf16x8 p0[2], p1[2];
      WAITV(8); RAWBAR();
      p0[0] = *(const bf16x8*)(smem + aB); p1[0] = *(const bf16x8*)(smem + aBo);
#pragma unroll
      for (int s = 0; s < 16; ++s) {
        if (s + 1 < 16) {
          if (s + 1 == 4) { WAITV(7); RAWBAR(); } else if (s + 1 == 8) { WAITV(4); RAWBAR(); } else if (s + 1 == 12) { WAITV(0); RAWBAR(); }
          p0[(s + 1) & 1] = *(const bf16x8*)(smem + aB + (s + 1) * 2048); p1[(s + 1) & 1] = *(const bf16x8*)(smem + aBo + (s + 1) * 2048);
        }
        SB();
        c0a = mfma32(asbf(w2[s]), p0[s & 1], c0a); c1a = mfma32(asbf(w2[s]), p1[s & 1], c1a);
        SB();
        if (s < 4) glds16(voc[3], sl + ROT(12 + s) * 1024, lb + ((half * 16 + 12 + s) * 2 + tt) * 1024);
        if (s >= 12 && w == 0) { if (s == 12) LDW(64, 52, 55, w3, w3p); else if (s == 13) LDW(64, 55, 58, w3, w3p); else if (s == 14) LDW(64, 58, 61, w3, w3p); else LDW(64, 61, 64, w3, w3p); }
        SB();
      }
    }
#pragma unroll
    for (int q = 0; q < 4; ++q) *(f32x4*)(exw + q * 1024) = (f32x4){c1a[4 * q], c1a[4 * q + 1], c1a[4 * q + 2], c1a[4 * q + 3]};
    BAR();
    {
      f32x16 z = c0a;
#pragma unroll
      for (int q = 0; q < 4; ++q) { f32x4 p = *(const f32x4*)(exr + q * 1024);
#pragma unroll
        for (int j = 0; j < 4; ++j) z[4 * q + j] += p[j]; }
      f32x2 ssum2 = {0.f, 0.f}, ssq2 = {0.f, 0.f};
#pragma unroll
      for (int sp = 0; sp < 2; ++sp) {
        u32x4 o;
#pragma unroll
        for (int j = 0; j < 4; ++j) {
          int r = 8 * sp + 2 * j;
          u32 p = pk2v((f32x2){bflo(xb[sp][j]), bfhi(xb[sp][j])} + (f32x2){z[r], z[r + 1]});
          o[j] = p; const f32x2 a = {bflo(p), bfhi(p)};
          ssum2 += a; ssq2 = __builtin_elementwise_fma(a, a, ssq2);
        }
        x1r[sp] = o;
        st16(rs, WS_X1 + (T * 32 + s_out + sp) * 1024 + lane * 16, o);
      }
      float ssum = swap32_add(ssum2[0] + ssum2[1]), ssq = swap32_add(ssq2[0] + ssq2[1]);
      if (lane < 32) __builtin_amdgcn_raw_buffer_store_b64((u32x2){__float_as_uint(ssum), __float_as_uint(ssq)}, rs,
                                                           WS_ST + ((T * 32 + lane) * 16 + ct * 2 + nh) * 8, 0, 16);
    }
    SB();
    LDW4(0, 12);
    WAITV(12);
    publish(ws, 3, me, tag);
  }
  const char* slh = ws + WS_HH + ((size_t)(panel * 2 + (w & 1)) * 112 + (w >> 1) * 56) * 1024;
  {
    const bf16x8 onesv = {(__bf16)1.f, (__bf16)1.f, (__bf16)1.f, (__bf16)1.f, (__bf16)1.f, (__bf16)1.f, (__bf16)1.f, (__bf16)1.f};
    f32x4 ca[4] = {};
#pragma unroll
    for (int s = 0; s < 16; ++s)
#pragma unroll
      for (int i = 0; i < 4; ++i) ca[i] = mfma16(asbf(w3[i * 16 + s]), onesv, ca[i]);
    SB();
#pragma unroll
    for (int i = 18; i < 34; ++i) glds16(lane * 16, ws + w4p + JL(i) * 1024, lb + 65536 + w * 16384 + (i - 18) * 1024);
    SB();
    wait_flags(ws, 3, lane < 8 ? panel * 8 + lane : -1, w, tag);
    u32x4 stv[4][2];
    const char* slx = ws + WS_X1 + (size_t)(panel * 2) * 32 * 1024;
    auto stg3 = [&](int c, int i) { const int s16 = 8 * c + 2 * w + (i >> 1), tt = i & 1;
      glds16(lane * 16, slx + (tt * 32 + 2 * ROT(4 * c) + 2 * w + (i >> 1)) * 1024, lb + (s16 * 2 + tt) * 1024); };
#pragma unroll
    for (int c = 0; c < 3; ++c)
#pragma unroll
      for (int i = 0; i < 4; ++i) stg3(c, i);
    SB();
    f32x4 acc[14];
#pragma unroll
    for (int i = 0; i < 14; ++i) acc[i] = (f32x4){0.f, 0.f, 0.f, 0.f};
    const int g = lane >> 4;
    const u32 aBa = ((g >> 1) * 2) * 1024 + ((lane & 15) + 32 * (g & 1)) * 16 + (w & 1) * 1024, aBb = aBa ^ 1024;
    bf16x8 bq[2][4];
    auto ldb = [&](int s, int k) {
#pragma unroll
      for (int tk = 0; tk < 4; ++tk) bq[k][tk] = *(const bf16x8*)(smem + ((tk >> 1) ? aBb : aBa) + s * 4096 + (tk & 1) * 256);
    };
    WAITV(8); RAWBAR();
    ldb(0, 0);
#pragma unroll
    for (int s = 0; s < 16; ++s) {
      if (s + 1 < 16) { if (s + 1 == 4) { WAITV(7); RAWBAR(); } else if (s + 1 == 8) { WAITV(10); RAWBAR(); } else if (s + 1 == 12) { WAITV(11); RAWBAR(); } }
      ldb((s + 1) & 15, (s + 1) & 1);
      SB();
#pragma unroll
      for (int i = 0; i < 2; ++i)
#pragma unroll
        for (int tk = 0; tk < 4; ++tk) acc[i * 4 + tk] = mfma16(asbf(w3[i * 16 + s]), bq[s & 1][tk], acc[i * 4 + tk]);
      SB();
      if (s < 4) stg3(3, s);
      else if (s < 8) {
        const int tk = s - 4;
#pragma unroll
        for (int q = 0; q < 2; ++q)
          stv[tk][q] = ld16(rs, (panel * 64 + (tk ^ (2 * (w & 1))) * 16 + (lane & 15)) * 128 + (lane >> 4) * 32 + q * 16, WS_ST);
      } else if (s < 14) { if (s == 8) LDW4(12, 13); else if (s == 9) LDW4(13, 14); else if (s == 10) LDW4(14, 15); else if (s == 11) LDW4(15, 16); else if (s == 12) LDW4(16, 17); else LDW4(17, 18); }
      SB();
    }
#pragma unroll
    for (int i = 18; i < 34; ++i) w4[i] = *(const u32x4*)(smem + 65536 + w * 16384 + (i - 18) * 1024 + lane * 16);
    float mean[4], rsc[4];
#pragma unroll
    for (int tk = 0; tk < 4; ++tk) {
      float a = __uint_as_float(stv[tk][0][0]) + __uint_as_float(stv[tk][0][2]) + __uint_as_float(stv[tk][1][0]) + __uint_as_float(stv[tk][1][2]);
      float q2 = __uint_as_float(stv[tk][0][1]) + __uint_as_float(stv[tk][0][3]) + __uint_as_float(stv[tk][1][1]) + __uint_as_float(stv[tk][1][3]);
      a = swap32_add(swap16_add(a)); q2 = swap32_add(swap16_add(q2));
      mean[tk] = a * (1.f / D);
      rsc[tk] = __builtin_amdgcn_rsqf(fmaxf(q2 * (1.f / D) - mean[tk] * mean[tk], 0.f) + 1e-5f);
    }
    auto gel = [&](int ti, int slot, int tk) -> u32x2 {
      const f32x4 c = ca[slot]; const float m = mean[tk], r = rsc[tk];
      return (u32x2){gelu_ln2(acc[ti][0], acc[ti][1], c[0], c[1], m, r), gelu_ln2(acc[ti][2], acc[ti][3], c[2], c[3], m, r)};
    };
    const int tkx = 2 * (w & 1);
    auto stpair = [&](u32x2 A, u32x2 B, int jA, int tkA, int jB, int tkB) {
      auto r0 = __builtin_amdgcn_permlane32_swap(A[0], B[0], false, false);
      auto r1 = __builtin_amdgcn_permlane32_swap(A[1], B[1], false, false);
      const int j = lane < 32 ? jA : jB, tk = (lane < 32 ? tkA : tkB) ^ tkx;
      st16(rs, WS_HH + ((panel * 2 + (tk >> 1)) * 112 + j) * 1024 + (16 * (tk & 1) + (lane & 15) + 32 * (g & 1)) * 16, (u32x4){r0[0], r1[0], r0[1], r1[1]});
    };
    const int j0 = ct * 14 + 3 * w, js = ct * 14 + 12 + (w >> 1);
    stpair(gel(0, 0, 0), gel(4, 1, 0), j0, 0, j0 + 1, 0); SB(); LDW4(34, 37);
    stpair(gel(1, 0, 1), gel(5, 1, 1), j0, 1, j0 + 1, 1); SB(); LDW4(37, 40);
    stpair(gel(2, 0, 2), gel(6, 1, 2), j0, 2, j0 + 1, 2); SB(); LDW4(40, 43);
    stpair(gel(3, 0, 3), gel(7, 1, 3), j0, 3, j0 + 1, 3); SB(); LDW4(43, 45);
#pragma unroll
    for (int s = 0; s < 16; ++s) {
      if (s + 1 < 16) ldb(s + 1, (s + 1) & 1);
      SB();
#pragma unroll
      for (int tk = 0; tk < 4; ++tk) acc[8 + tk] = mfma16(asbf(w3[32 + s]), bq[s & 1][tk], acc[8 + tk]);
      acc[12] = mfma16(asbf(w3[48 + s]), bq[s & 1][0], acc[12]); acc[13] = mfma16(asbf(w3[48 + s]), bq[s & 1][1], acc[13]);
      SB();
      if (s < 5) { if (s == 0) LDW4(45, 46); else if (s == 1) LDW4(46, 47); else if (s == 2) LDW4(47, 48); else if (s == 3) LDW4(48, 49); else LDW4(49, 50); }
      else if (s == 5) { WAITV(7); publish(ws, 4, me, tag); LDW4(50, 51); }
      else if (s < 11) { if (s == 6) LDW4(51, 52); else if (s == 7) LDW4(52, 53); else if (s == 8) LDW4(53, 54); else if (s == 9) LDW4(54, 55); else LDW4(55, 56); }
      SB();
    }
    wait_flags(ws, 4, lane < 8 ? panel * 8 + lane : -1, w, tag);
#define STAGE4(c, s4) glds16(lane * 16, slh + JL((c) * 4 + (s4)) * 1024, lb + ((c) & 7) * 16384 + (((w >> 1) * 4 + (s4)) * 2 + (w & 1)) * 1024)
#pragma unroll
    for (int c = 0; c < 3; ++c)
#pragma unroll
      for (int s4 = 0; s4 < 4; ++s4) STAGE4(c, s4);
    SB();
    stpair(gel(8, 2, 0), gel(9, 2, 1), j0 + 2, 0, j0 + 2, 1);
    stpair(gel(10, 2, 2), gel(11, 2, 3), j0 + 2, 2, j0 + 2, 3);
    stpair(gel(12, 3, 0), gel(13, 3, 1), js, 0, js, 1);
    SB();
  }
  {
    f32x16 c0a = {}, c1a = {};
    const u32 aB = kh * 8 * 1024 + lane * 16 + tto * 1024, aBo = aB ^ 1024;
    bf16x8 p0[2], p1[2];
    WAITV(11);
    RAWBAR();
    p0[0] = *(const bf16x8*)(smem + aB); p1[0] = *(const bf16x8*)(smem + aBo);
#pragma unroll
    for (int s = 0; s < 56; ++s) {
      if (s + 1 < 56) {
        const int c = (s + 1) >> 2;
        if (((s + 1) & 3) == 0) {
          if (c == 1) WAITV(10); else if (c + 2 < 14) WAITV(7); else if (c + 2 == 14) WAITV(4); else WAITV(0);
          if (c == 2) publish(ws, 5, me, tag);
          else if (c == 5) wait_flags(ws, 5, lane < 8 ? panel * 8 + lane : -1, w, tag);
          else RAWBAR();
        }
        const u32 sa = (c & 7) * 16384 + ((s + 1) & 3) * 2048;
        p0[(s + 1) & 1] = *(const bf16x8*)(smem + sa + aB); p1[(s + 1) & 1] = *(const bf16x8*)(smem + sa + aBo);
      }
      SB();
      c0a = mfma32(asbf(w4[s]), p0[s & 1], c0a); c1a = mfma32(asbf(w4[s]), p1[s & 1], c1a);
      SB();
      if ((s >> 2) + 3 < 14) STAGE4((s >> 2) + 3, s & 3);
      SB();
    }
#pragma unroll
    for (int q = 0; q < 4; ++q) *(f32x4*)(exw + q * 1024) = (f32x4){c1a[4 * q], c1a[4 * q + 1], c1a[4 * q + 2], c1a[4 * q + 3]};
    BAR();
    {
      f32x16 z = c0a;
#pragma unroll
      for (int q = 0; q < 4; ++q) { f32x4 p = *(const f32x4*)(exr + q * 1024);
#pragma unroll
        for (int j = 0; j < 4; ++j) z[4 * q + j] += p[j]; }
      u32x2 o[4];
#pragma unroll
      for (int qq = 0; qq < 4; ++qq) {
        const int sp = qq >> 1, jj = (qq & 1) * 2;
#pragma unroll
        for (int e = 0; e < 2; ++e) {
          const u32 xp = x1r[sp][jj + e], hp = hres[sp][jj + e];
          o[qq][e] = pk2v((f32x2){bflo(xp), bfhi(xp)} + (f32x2){bflo(hp), bfhi(hp)} + (f32x2){z[4 * qq + 2 * e], z[4 * qq + 2 * e + 1]});
        }
      }
      char* op = (char*)out + ((size_t)(T * 32 + (lane & 31)) * D + ct * 64 + nh * 32) * 2 + h * 16;
#pragma unroll
      for (int pr = 0; pr < 2; ++pr) {
        auto r0 = __builtin_amdgcn_permlane32_swap(o[2 * pr][0], o[2 * pr + 1][0], false, false);
        auto r1 = __builtin_amdgcn_permlane32_swap(o[2 * pr][1], o[2 * pr + 1][1], false, false);
        *(u32x4*)(op + pr * 32) = (u32x4){r0[0], r1[0], r0[1], r1[1]};
      }
    }
  }
  asm volatile("km_code_end:");
}

extern "C" void kernel_launch(void* const* d_in, const int* in_sizes, int n_in, void* d_out, int out_size, void* d_ws, size_t ws_size,
                              hipStream_t stream) {
  (void)hipFuncSetAttribute((const void*)kmain, hipFuncAttributeMaxDynamicSharedMemorySize, LDS_BYTES);
  kmain<<<256, 256, LDS_BYTES, stream>>>((const u16*)d_in[0], (const u16*)d_in[1], (const u16*)d_in[2], (const u16*)d_in[3], (const u16*)d_in[4],
                                         (const u16*)d_in[5], (const u16*)d_in[6], (const u16*)d_in[8], (const u16*)d_in[9], (const u16*)d_in[10],
                                         (const u16*)d_in[11], (char*)d_ws, (u16*)d_out);
}
```

```cpp
#include <hip/hip_runtime.h>
#include <hip/hip_bf16.h>
#include <stdint.h>

typedef unsigned short u16;
typedef unsigned int u32;
typedef unsigned long long u64;
typedef u32 u32x2 __attribute__((ext_vector_type(2)));
typedef u32 u32x4 __attribute__((ext_vector_type(4)));
typedef float f32x2 __attribute__((ext_vector_type(2)));
typedef float f32x4 __attribute__((ext_vector_type(4)));
typedef float f32x16 __attribute__((ext_vector_type(16)));
typedef short s16x4 __attribute__((ext_vector_type(4)));
typedef __bf16 bf16x2 __attribute__((ext_vector_type(2)));
typedef __bf16 bf16x8 __attribute__((ext_vector_type(8)));
#define LAS __attribute__((address_space(3)))
#define GAS __attribute__((address_space(1)))
#define DI __device__ __forceinline__
typedef GAS u64 gu64;
#define RLX_AGENT __ATOMIC_RELAXED, __HIP_MEMORY_SCOPE_AGENT

constexpr int L = 2048, D = 512, NS = 128, F = 1792;
constexpr int KR_LEN = 2112;
constexpr int KR_OFF = 2063;

constexpr u32 WS_UX = 0;
constexpr u32 WS_HP = 4u << 20;
constexpr u32 WS_H1 = 6u << 20;
constexpr u32 WS_X1 = 9u << 20;
constexpr u32 WS_ST = 11u << 20;
constexpr u32 WS_HH = 12u << 20;
constexpr u32 WS_WT1 = 20u << 20;
constexpr u32 WS_WT2 = WS_WT1 + (512u << 10);
constexpr u32 WS_WT4 = 21u << 20;
constexpr u32 WS_WT3 = 23u << 20;
constexpr u32 WS_FL = 25u << 20;
constexpr u32 WS_SEED = WS_FL + 6 * 256 * 128;
constexpr u32 WS_SPAN = 32u << 20;

DI float bf2f(u32 v) { return __uint_as_float(v << 16); }
DI float bflo(u32 v) { return __uint_as_float(v << 16); }
DI float bfhi(u32 v) { return __uint_as_float(v & 0xffff0000u); }
DI u32 pk2(float lo, float hi) { f32x2 v = {lo, hi}; bf16x2 p = __builtin_convertvector(v, bf16x2); return __builtin_bit_cast(u32, p); }
DI u32 pk2v(f32x2 v) { return __builtin_bit_cast(u32, __builtin_convertvector(v, bf16x2)); }
DI float rbf(float f) { return bflo(pk2(f, 0.f) & 0xffffu); }
template <int CTRL, int RMASK>
DI float dppmov(float v) { return __int_as_float(__builtin_amdgcn_update_dpp(0, __float_as_int(v), CTRL, RMASK, 0xf, false)); }
DI float wave_sum(float v) {
  v += dppmov<0x111, 0xf>(v); v += dppmov<0x112, 0xf>(v); v += dppmov<0x114, 0xf>(v); v += dppmov<0x118, 0xf>(v);
  v += dppmov<0x142, 0xa>(v); v += dppmov<0x143, 0xc>(v);
  return __int_as_float(__builtin_amdgcn_readlane(__float_as_int(v), 63));
}
DI float swap32_add(float v) { auto r = __builtin_amdgcn_permlane32_swap(__float_as_uint(v), __float_as_uint(v), false, false); return __uint_as_float(r[0]) + __uint_as_float(r[1]); }
DI float swap16_add(float v) { auto r = __builtin_amdgcn_permlane16_swap(__float_as_uint(v), __float_as_uint(v), false, false); return __uint_as_float(r[0]) + __uint_as_float(r[1]); }
DI f32x2 gelu2(f32x2 z) {
  const float c0 = -2.f * 0.7978845608028654f * 1.4426950408889634f, c1 = c0 * 0.044715f;
  const f32x2 C0 = {c0, c0}, C1 = {c1, c1}, one = {1.f, 1.f};
  f32x2 a = z * __builtin_elementwise_fma(z * z, C1, C0);
  f32x2 e = {__builtin_amdgcn_exp2f(a[0]), __builtin_amdgcn_exp2f(a[1])};
  e = e + one;
  f32x2 r = {__builtin_amdgcn_rcpf(e[0]), __builtin_amdgcn_rcpf(e[1])};
  return z * r;
}
DI u32 gelu_ln2(float a0, float a1, float c0, float c1, float m, float r) {
  const f32x2 M = {-m, -m}, R = {r, r};
  f32x2 z = __builtin_elementwise_fma((f32x2){c0, c1}, M, (f32x2){a0, a1}) * R;
  return pk2v(gelu2(z));
}
DI void glds16(u32 voff, const void* sbase, u32 lds) {
  asm volatile("s_mov_b32 m0, %2\n\ts_nop 0\n\tglobal_load_lds_dwordx4 %0, %1 sc1" :: "v"(voff), "s"(sbase), "s"(lds) : "memory");
}
DI u32 lds_base(char* smem) { return (u32)(uintptr_t)(LAS char*)smem; }
#define WAITV(n) asm volatile("s_waitcnt vmcnt(" #n ")" ::: "memory")
#define WAITV_T(n) asm volatile("s_waitcnt vmcnt(%0)" :: "i"(n) : "memory")
#define SB() __builtin_amdgcn_sched_barrier(0)
#define RAWBAR() do { SB(); __builtin_amdgcn_s_barrier(); SB(); } while (0)
#define BAR() do { SB(); asm volatile("s_waitcnt lgkmcnt(0)" ::: "memory"); __builtin_amdgcn_s_barrier(); asm volatile("" ::: "memory"); SB(); } while (0)
DI bf16x8 tr_frag(char* smem, u32 a0, u32 a1) {
  s16x4 a = __builtin_amdgcn_ds_read_tr16_b64_v4i16((LAS s16x4*)(smem + a0));
  s16x4 b = __builtin_amdgcn_ds_read_tr16_b64_v4i16((LAS s16x4*)(smem + a1));
  return __builtin_bit_cast(bf16x8, __builtin_shufflevector(a, b, 0, 1, 2, 3, 4, 5, 6, 7));
}
DI f32x16 mfma32(bf16x8 a, bf16x8 b, f32x16 c) { return __builtin_amdgcn_mfma_f32_32x32x16_bf16(a, b, c, 0, 0, 0); }
DI f32x4 mfma16(bf16x8 a, bf16x8 b, f32x4 c) { return __builtin_amdgcn_mfma_f32_16x16x32_bf16(a, b, c, 0, 0, 0); }
DI bf16x8 asbf(u32x4 v) { return __builtin_bit_cast(bf16x8, v); }
typedef __amdgpu_buffer_rsrc_t rsrc_t;
DI u32x4 ld16(rsrc_t rs, u32 voff, u32 soff) { return __builtin_amdgcn_raw_buffer_load_b128(rs, voff, soff, 16); }
DI void st16(rsrc_t rs, u32 off, u32x4 v) { __builtin_amdgcn_raw_buffer_store_b128(v, rs, off, 0, 16); }
template <int N, int LO, int HI>
DI void ldw_(u32x4 (&a)[N], rsrc_t rs, u32 base, int lane, int rot) {
#pragma unroll
  for (int i = LO; i < HI; ++i) a[i] = ld16(rs, lane * 16, base + ((i & ~15) + (((i & 15) + rot) & 15)) * 1024);
  SB();
}
#define LDW(N, LO, HI, arr, base) ldw_<N, LO, HI>(arr, rs, base, lane, rot)

DI u32 img128_srcoff(int lane) { return ((((lane >> 2) & 1) ^ ((lane >> 4) & 1)) * 64) + (lane & 3) * 16; }
DI u32 img128_traddr(int lane, int ct) {
  int h = lane >> 5, blk = (lane >> 4) & 1, q = (lane & 15) >> 2, p = lane & 3;
  return (4 * h + q) * 128 + ((ct ^ ((q >> 1) & 1)) * 64) + blk * 32 + p * 8;
}

struct KTab { u16 v[KR_LEN + 64]; };
constexpr int bf16_round_pos(int t) {
  if (t < 256) return t;
  int sh = t < 512 ? 1 : t < 1024 ? 2 : 3;
  int q = t >> sh, r = t & ((1 << sh) - 1), half = 1 << (sh - 1);
  if (r > half || (r == half && (q & 1))) ++q;
  return q << sh;
}
constexpr KTab make_ktab() {
  KTab t{};
  for (int i = 0; i < KR_LEN + 64; ++i) {
    int tau = KR_OFF - i; int idx = 1024;
    if (tau >= 0 && tau < L) {
      int p = bf16_round_pos(tau); int m = 0, col = 0;
      if (p < 256) { m = p & 7; col = p >> 3; }
      else if (p < 512) { m = 8 + ((p & 7) >> 1); col = (p - 256) >> 3; }
      else if (p < 1024) { m = 12 + 2 * ((p - 512) >> 8) + ((p >> 2) & 1); col = (p & 255) >> 3; }
      else { m = 16 + ((p - 1024) >> 8); col = (p & 255) >> 3; }
      idx = m * 32 + col;
    }
    t.v[i] = (u16)idx;
  }
  return t;
}
__device__ const KTab g_ktab = make_ktab();

struct cmx { f32x2 a, b; };
DI cmx cmx_of(f32x2 v) { return {v, (f32x2){-v[1], v[0]}}; }
DI f32x2 cmul2(f32x2 e, const cmx& m) { return __builtin_elementwise_fma((f32x2){e[1], e[1]}, m.b, (f32x2){e[0], e[0]} * m.a); }
DI f32x2 cexp_p(float ar, float ai_rev, float p) {
  float e = __builtin_amdgcn_exp2f(ar * p * 1.4426950408889634f);
  float rv = __builtin_amdgcn_fractf(ai_rev * p);
  return (f32x2){e * __builtin_amdgcn_cosf(rv), e * __builtin_amdgcn_sinf(rv)};
}

constexpr int KC_STR = 4416, KC_CH = 8 * KC_STR;
constexpr int U_QS = 1568, U_CH = 4 * U_QS;
constexpr int LD_U = 2 * KC_CH;
constexpr int LD_X = LD_U + 2 * U_CH;
constexpr int LD_KV = 96 * 1024;
constexpr int LD_LN = 128 * 1024;
constexpr int LD_KS = 144 * 1024;
constexpr int LD_EX = 128 * 1024;
constexpr int LD_SM = LD_EX + 16384;
constexpr int LD_CS1 = LD_SM + 1024, LD_CS3 = LD_CS1 + 1024;
constexpr int LD_PF = 144 * 1024 + 2 * 4224 + 256;
constexpr int LDS_BYTES = LD_PF + 1024;
static_assert(LD_X + 2 * U_CH <= LD_KV && LD_LN + 16384 <= LD_KS && LD_CS3 + 1024 <= LD_PF && (LD_PF & 15) == 0 && LDS_BYTES <= 160 * 1024, "lds");

constexpr unsigned SPIN_CAP = 1u << 18;
constexpr u64 TAG_INC = 0x9E3779B97F4A7C15ull;

DI void publish(char* ws, int phase, int id, u64 tag) {
  RAWBAR();
  if (threadIdx.x == 0) __hip_atomic_store((gu64*)(ws + WS_FL + (phase * 256 + id) * 128), tag, RLX_AGENT);
}
DI void wait_flags(char* ws, int phase, int idx, int w, u64 tag) {
  if (w == 0) {
    for (unsigned spins = 0; spins < SPIN_CAP; ++spins) {
      u64 f = tag;
      if (idx >= 0) f = __hip_atomic_load((gu64*)(ws + WS_FL + (phase * 256 + idx) * 128), RLX_AGENT);
      if (__all(f == tag)) break;
    }
  }
  RAWBAR();
}
DI void wait_all(char* ws, int phase, int w, int lane, u64 tag) {
  if (w == 0) {
    gu64* fp = (gu64*)(ws + WS_FL + (phase * 256 + lane) * 128);
    for (unsigned spins = 0; spins < SPIN_CAP; ++spins) {
      u64 f0 = __hip_atomic_load(fp, RLX_AGENT), f1 = __hip_atomic_load(fp + 64 * 16, RLX_AGENT);
      u64 f2 = __hip_atomic_load(fp + 128 * 16, RLX_AGENT), f3 = __hip_atomic_load(fp + 192 * 16, RLX_AGENT);
      if (__all(f0 == tag && f1 == tag && f2 == tag && f3 == tag)) break;
    }
  }
  RAWBAR();
}

#define NW3A 8

__global__ void __launch_bounds__(256, 1) kmain(const u16* __restrict__ x, const u16* __restrict__ logA, const u16* __restrict__ Aim,
                                                const u16* __restrict__ Cr, const u16* __restrict__ Ci, const u16* __restrict__ logdt,
                                                const u16* __restrict__ dskip, const u16* __restrict__ w1g, const u16* __restrict__ w2g,
                                                const u16* __restrict__ w3g, const u16* __restrict__ w4g, char* ws, u16* out) {
  extern __shared__ __attribute__((aligned(16))) char smem[];
  const int tid = threadIdx.x, lane = tid & 63, w = __builtin_amdgcn_readfirstlane(tid >> 6);
  const int b = blockIdx.x, xcd = b & 7, idx = b >> 3;
  const u32 lb = lds_base(smem);
  const rsrc_t rs = __builtin_amdgcn_make_buffer_rsrc(ws, 0, WS_SPAN, 0x00020000);
  const int h = lane >> 5;
  {
    u64 pc; u32 clen;
    asm volatile("s_getpc_b64 %0\nkm_code_begin:\n\ts_mov_b32 %1, km_code_end-km_code_begin" : "=s"(pc), "=s"(clen));
    const u32 chunk = (idx * 4 + w) * 1024;
    if (chunk < clen) {
      const u32 vo = min(chunk + lane * 16, clen - 16) & ~15u;
      asm volatile("s_mov_b32 m0, %2\n\ts_nop 0\n\tglobal_load_lds_dwordx4 %0, %1" :: "v"(vo), "s"(pc & ~15ull), "s"(lb + LD_PF) : "memory");
    }
    if (w == 3) {
      const u32 vo = (lane & 15) << 21;
      asm volatile("s_mov_b32 m0, %2\n\ts_nop 0\n\tglobal_load_lds_dword %0, %1" :: "v"(vo), "s"(ws), "s"(lb + LD_PF) : "memory");
    }
  }
  const int c0 = 2 * (xcd * 32 + idx);
  const int chl = w >> 1, ih = w & 1, ch = c0 + chl;
  u32 p_logA = logA[ch * NS + 64 * ih + lane], p_Aim = Aim[ch * NS + 64 * ih + lane], p_Cr = Cr[ch * NS + 64 * ih + lane], p_Ci = Ci[ch * NS + 64 * ih + lane];
  u32 p_dt = *(const GAS u16*)(logdt + ch + (lane & 0));
  SB();
  u32x4 xv[2];
#pragma unroll
  for (int r = 0; r < 2; ++r) xv[r] = *(const GAS u32x4*)(x + (size_t)(8 * b + 2 * w + r) * D + lane * 8);
  const u64 seed_v = __hip_atomic_load((gu64*)(ws + WS_SEED), RLX_AGENT);
  SB();
  u64 tag;

  const int panel = xcd * 4 + (idx & 3), ct = idx >> 2, me = panel * 8 + ct;
  const int kh = w >> 1, nh = w & 1, tto = kh ^ nh;
  const u32 w1p = WS_WT1 + ((ct * 2 + nh) * 2 + kh) * 16384, w2p = WS_WT2 + ((ct * 2 + nh) * 2 + kh) * 16384;
  const u32 w3p = WS_WT3 + (ct * 4 + w) * 65536, w4p = WS_WT4 + ((ct * 2 + nh) * 2 + kh) * 56 * 1024;
  u32x4 w1[16], w2[16], w3[64];
  const int rot = (ct & 3) * 4;
#define ROT(s) (((s) + rot) & 15)
  u32x4 w4[56];
#define JL(i) ((i) < 32 ? 14 * (((((i) >> 2) + ct) & 7) >> 1) + 6 * ((((i) >> 2) + ct) & 1) + ((i) & 1) + 3 * (((i) >> 1) & 1) \
                        : 14 * (((((i) - 32) / 6) + ct) & 3) + ((((i) - 32) % 6) < 4 ? (((i) - 32) % 6) * 3 + 2 : (((i) - 32) % 6) + 8))
#define LDW4(LO, HI) do { _Pragma("unroll") for (int i_ = (LO); i_ < (HI); ++i_) w4[i_] = ld16(rs, lane * 16, w4p + JL(i_) * 1024); SB(); } while (0)

  {
    u32x4 ktab[3];
#pragma unroll
    for (int it = 0; it < 3; ++it) { int U = 63 * (it * 4 + w) + lane; int u8 = U >= KR_LEN / 8 ? U - KR_LEN / 8 : U; if (u8 >= KR_LEN / 8) u8 = 0; ktab[it] = *(const GAS u32x4*)(g_ktab.v + u8 * 8); }
    if (tid < 2) *(float*)(smem + LD_KS + tid * 4224 + 4096) = 0.f;
    int u = w * 256 + b; const bool has_unit = u < 576;
    const u16* W = w1g; int N = D, ku = 0, nu = 0, kind = 0;
    if (u < 64) { W = w1g; ku = u >> 3; nu = u & 7; kind = 0; }
    else if (u < 128) { u -= 64; W = w2g; ku = u >> 3; nu = u & 7; kind = 1; }
    else if (u < 352) { u -= 128; W = w4g; ku = u >> 3; nu = u & 7; kind = 2; }
    else if (u < 576) { u -= 352; W = w3g; N = F; ku = u / 28; nu = u % 28; kind = 3; }
    u32x4 wv[8];
    {
      const u16* src = W + (size_t)(ku * 64 + (lane >> 3)) * N + nu * 64 + (lane & 7) * 8;
#pragma unroll
      for (int i = 0; i < 8; ++i) wv[i] = *(const GAS u32x4*)(src + (size_t)i * 8 * N);
    }
    SB();
    asm volatile("" : "+v"(p_logA), "+v"(p_Aim), "+v"(p_Cr), "+v"(p_Ci), "+v"(p_dt) :: "memory");
    const float l2e = 1.4426950408889634f;
    const float dt = rbf(__builtin_amdgcn_exp2f(bf2f(p_dt) * l2e));
    const float ar = -rbf(__builtin_amdgcn_exp2f(bf2f(p_logA) * l2e)) * dt;
    const float ai = bf2f(p_Aim) * dt * 0.15915494309189535f;
    const f32x2 cc = {2.f * bf2f(p_Cr) * dt, 2.f * bf2f(p_Ci) * dt};

    {
      const f32x2 c = cc;
      char* rowB = smem + w * (53 * 272) + lane * 4;
      char* rowA = rowB + 32 * 272;
      const cmx W1 = cmx_of(cexp_p(ar, ai, 1.f)), W8 = cmx_of(cexp_p(ar, ai, 8.f)), E256 = cmx_of(cexp_p(ar, ai, 256.f));
      const f32x2 e64 = cexp_p(ar, ai, 64.f), e128 = cexp_p(ar, ai, 128.f), e192 = cexp_p(ar, ai, 192.f);
      const f32x2 cj = {1.f, -1.f};
      f32x2 e0 = {1.f, 0.f}, e1 = e64, e2 = e128, e3 = e192, ea = c;
#pragma unroll
      for (int col = 0; col < 8; ++col) {
        *(u32*)(rowB + col * 272) = pk2v(e0); *(u32*)(rowB + (8 + col) * 272) = pk2v(e1);
        *(u32*)(rowB + (16 + col) * 272) = pk2v(e2); *(u32*)(rowB + (24 + col) * 272) = pk2v(e3);
        *(u32*)(rowA + col * 272) = pk2v(ea * cj);
        e0 = cmul2(e0, W8); e1 = cmul2(e1, W8); e2 = cmul2(e2, W8); e3 = cmul2(e3, W8); ea = cmul2(ea, W1);
      }
      const f32x2 w1v = W1.a, w2v = cmul2(w1v, W1); const cmx W2 = cmx_of(w2v); const cmx W4 = cmx_of(cmul2(w2v, W2));
      const f32x2 c256 = cmul2(c, E256), c512 = cmul2(c256, E256), c768 = cmul2(c512, E256), c1024 = cmul2(c768, E256);
      f32x2 eb = c256, ec = c1024;
#pragma unroll
      for (int m = 0; m < 4; ++m) {
        *(u32*)(rowA + (8 + m) * 272) = pk2v(eb * cj); *(u32*)(rowA + (16 + m) * 272) = pk2v(ec * cj);
        eb = cmul2(eb, W2); ec = cmul2(ec, E256);
      }
      *(u32*)(rowA + 20 * 272) = pk2v(ec * cj);
      *(u32*)(rowA + 12 * 272) = pk2v(c512 * cj); *(u32*)(rowA + 13 * 272) = pk2v(cmul2(c512, W4) * cj);
      *(u32*)(rowA + 14 * 272) = pk2v(c768 * cj); *(u32*)(rowA + 15 * 272) = pk2v(cmul2(c768, W4) * cj);
    }
    f32x16 kacc = {};
    {
      const int m = lane & 31;
      bf16x8 af[8], bf[8];
#pragma unroll
      for (int s = 0; s < 8; ++s) {
        af[s] = *(const bf16x8*)(smem + w * (53 * 272) + (32 + m) * 272 + 32 * s + 16 * h);
        bf[s] = *(const bf16x8*)(smem + w * (53 * 272) + m * 272 + 32 * s + 16 * h);
      }
      SB();
#pragma unroll
      for (int s = 0; s < 8; ++s) kacc = mfma32(af[s], bf[s], kacc);
    }
    asm volatile("" :: "v"(ktab[0][0]), "v"(ktab[1][0]), "v"(ktab[2][0]));
    tag = (((u64)(u32)__builtin_amdgcn_readfirstlane((u32)(seed_v >> 32)) << 32) | (u32)__builtin_amdgcn_readfirstlane((u32)seed_v)) + TAG_INC;
    {
      float uo[2][8], v[2][8], sm[2], sq[2];
#pragma unroll
      for (int r = 0; r < 2; ++r) {
#pragma unroll
        for (int j = 0; j < 4; ++j) { v[r][2 * j] = bflo(xv[r][j]); v[r][2 * j + 1] = bfhi(xv[r][j]); }
        sm[r] = ((v[r][0] + v[r][1]) + (v[r][2] + v[r][3])) + ((v[r][4] + v[r][5]) + (v[r][6] + v[r][7]));
      }
#pragma unroll
      for (int r = 0; r < 2; ++r) sm[r] = wave_sum(sm[r]) * (1.f / D);
#pragma unroll
      for (int r = 0; r < 2; ++r) {
        sq[r] = 0.f;
#pragma unroll
        for (int j = 0; j < 8; ++j) { v[r][j] -= sm[r]; sq[r] = __builtin_fmaf(v[r][j], v[r][j], sq[r]); }
      }
#pragma unroll
      for (int r = 0; r < 2; ++r) {
        const float rsb = __builtin_amdgcn_rsqf(wave_sum(sq[r]) * (1.f / D) + 1e-5f);
#pragma unroll
        for (int j = 0; j < 8; ++j) uo[r][j] = v[r][j] * rsb;
      }
      const int sw = lane & 7;
#pragma unroll
      for (int j = 0; j < 8; ++j) {
        char* row = smem + LD_LN + (lane * 8 + j) * 32;
        const u32 x0 = xv[0][j >> 1], x1 = xv[1][j >> 1];
        *(u32*)(row + ((w ^ sw) * 4)) = pk2(uo[0][j], uo[1][j]);
        *(u32*)(row + (((4 + w) ^ sw) * 4)) = (j & 1) ? ((x0 >> 16) | (x1 & 0xffff0000u)) : ((x0 & 0xffffu) | (x1 << 16));
      }
    }
    BAR();
#pragma unroll
    for (int it = 0; it < 4; ++it) {
      const int unit = it * 256 + tid, cc = unit >> 1, half = unit & 1, s = (cc >> 3) & 7;
      u32x4 v = *(const u32x4*)(smem + LD_LN + cc * 32 + (((4 * half) ^ (s & 4)) * 4));
      if (s & 1) v = (u32x4){v[1], v[0], v[3], v[2]};
      if (s & 2) v = (u32x4){v[2], v[3], v[0], v[1]};
      st16(rs, WS_UX + b * 16384 + unit * 16, v);
    }
    {
      char* img = smem + LD_KV + w * 8192; const u32 imgo = LD_KV + w * 8192;
      const int pc = lane & 7;
#pragma unroll
      for (int i = 0; i < 8; ++i) {
        const int r = i * 8 + (lane >> 3);
        const int pp = (kind == 3) ? ((((pc >> 1) ^ ((r >> 1) & 3)) << 1) | (pc & 1)) : ((((pc >> 2) ^ ((r >> 1) & 1)) << 2) | (pc & 3));
        *(u32x4*)(img + r * 128 + pp * 16) = wv[i];
      }
      if (!has_unit) {} else
      if (kind != 3) {
        const int ksh = kind == 2 ? 56 : 16;
        const u32 dst = kind == 0 ? WS_WT1 : kind == 1 ? WS_WT2 : WS_WT4;
#pragma unroll
        for (int hn = 0; hn < 2; ++hn) {
          const u32 a = imgo + img128_traddr(lane, hn);
#pragma unroll
          for (int s4 = 0; s4 < 4; ++s4) {
            bf16x8 f = tr_frag(smem, a + s4 * 2048, a + s4 * 2048 + 1024);
            const int s = ku * 4 + s4; const int k2 = s >= ksh; const int sl = s - k2 * ksh;
            st16(rs, dst + (((nu * 2 + hn) * 2 + k2) * ksh + sl) * 1024 + lane * 16, __builtin_bit_cast(u32x4, f));
          }
        }
      } else {
        const int g = lane >> 4, q = (lane & 15) >> 2, p = lane & 3;
#pragma unroll
        for (int c16 = 0; c16 < 4; ++c16) {
          const int j = nu * 4 + c16; const int tc = j / 14, r14 = j % 14;
#pragma unroll
          for (int sp = 0; sp < 2; ++sp) {
            const int r0 = 32 * sp + 16 * (g >> 1) + 4 * (g & 1) + q, r1 = r0 + 8;
            bf16x8 f = tr_frag(smem, imgo + r0 * 128 + ((c16 ^ ((r0 >> 1) & 3)) * 32) + p * 8, imgo + r1 * 128 + ((c16 ^ ((r1 >> 1) & 3)) * 32) + p * 8);
            const u32 dst = WS_WT3 + tc * 4 * 65536 + (ku * 2 + sp) * 1024 + lane * 16;
            if (r14 < 12) st16(rs, dst + (r14 / 3) * 65536 + (r14 % 3) * 16384, __builtin_bit_cast(u32x4, f));
            else { const int w0 = 2 * (r14 - 12); st16(rs, dst + w0 * 65536 + 3 * 16384, __builtin_bit_cast(u32x4, f)); st16(rs, dst + (w0 + 1) * 65536 + 3 * 16384, __builtin_bit_cast(u32x4, f)); }
          }
        }
      }
    }
    if (b >= 252) {
      const u32 dst = WS_H1 + ((b & 2) ? 65u * 32 * 1024 : 0u) + (b & 1) * 16384 + tid * 16; const u32x4 z = {0, 0, 0, 0};
#pragma unroll
      for (int i = 0; i < 4; ++i) st16(rs, dst + i * 4096, z);
    }
    BAR();
    {
      float* kv = (float*)(smem + LD_KV + w * 4096);
      const int col = lane & 31;
#pragma unroll
      for (int r = 0; r < 16; ++r) kv[((r & 3) + 8 * (r >> 2) + 4 * h) * 32 + col] = kacc[r];
    }
    BAR();
#pragma unroll
    for (int it = 0; it < 2; ++it) {
      const int i4 = it * 256 + tid, c2 = i4 >> 8, e4 = i4 & 255;
      const f32x4 a = *(const f32x4*)(smem + LD_KV + (2 * c2) * 4096 + e4 * 16), bq = *(const f32x4*)(smem + LD_KV + (2 * c2 + 1) * 4096 + e4 * 16);
      *(f32x4*)(smem + LD_KS + c2 * 4224 + e4 * 16) = a + bq;
    }
    { const int c2 = tid >> 7, q = (tid >> 5) & 3, cc = tid & 31; *(u32x4*)(smem + LD_U + c2 * U_CH + q * U_QS + cc * 16) = (u32x4){0, 0, 0, 0}; }
    asm volatile("s_waitcnt vmcnt(0) lgkmcnt(0)" ::: "memory");
    publish(ws, 0, b, tag);
#pragma unroll
    for (int it = 0; it < 3; ++it) {
      const int U = 63 * (it * 4 + w) + lane;
      const int c2 = U >= KR_LEN / 8, u8 = U - c2 * (KR_LEN / 8);
      const float* kv = (const float*)(smem + LD_KS + (c2 & 1) * 4224);
      u32 d[8];
#pragma unroll
      for (int j = 0; j < 4; ++j) d[j] = pk2(kv[ktab[it][j] & 0xffffu], kv[ktab[it][j] >> 16]);
#pragma unroll
      for (int j = 0; j < 4; ++j) d[4 + j] = __builtin_amdgcn_update_dpp(0, d[j], 0x130, 0xf, 0xf, false);
      if (lane < 63 && U < 2 * (KR_LEN / 8)) {
        u32 a[7];
#pragma unroll
        for (int k = 0; k < 7; ++k) a[k] = __builtin_amdgcn_alignbyte(d[k + 1], d[k], 2);
        char* dst = smem + c2 * KC_CH + u8 * 16;
#pragma unroll
        for (int k = 0; k < 4; ++k) {
          *(u32x4*)(dst + (7 - 2 * k) * KC_STR) = (u32x4){d[k], d[k + 1], d[k + 2], d[k + 3]};
          *(u32x4*)(dst + (6 - 2 * k) * KC_STR) = (u32x4){a[k], a[k + 1], a[k + 2], a[k + 3]};
        }
      }
    }
  }
  wait_all(ws, 0, w, lane, tag);
  if (b == 0 && tid == 0) __hip_atomic_store((gu64*)(ws + WS_SEED), tag, RLX_AGENT);
  {
    const float dsk = bf2f(dskip[ch]);
    {
      u32x4 gv[4];
#pragma unroll
      for (int i = 0; i < 4; ++i) gv[i] = ld16(rs, (64 * w + 16 * i + (lane >> 2)) * 16384 + (lane & 3) * 16, WS_UX + c0 * 32);
#pragma unroll
      for (int i = 0; i < 4; ++i) {
        const int tb = 64 * w + 16 * i + (lane >> 2), pc = lane & 3;
        *(u32x4*)(smem + ((pc & 1) ? LD_X : LD_U) + (pc >> 1) * U_CH + (tb & 3) * U_QS + (32 + (tb >> 2)) * 16) = gv[i];
      }
    }
    BAR();
    const int n = lane & 31;
    f32x16 acc0 = {}, acc1 = {};
    {
      const u32 abase = chl * KC_CH + (n & 7) * KC_STR + 16 * (257 + h - (n >> 3)) - 32 * (32 * ih - 1) - 32 * 95;
      const u32 ubase = LD_U + chl * U_CH + h * U_QS + (32 + n - 16 * ih) * 16 - 16 * 16;
      bf16x8 A1[3], A2[3], b0[3], b1[3];
      auto ld = [&](int ii) {
        const int k = ii % 3;
        A1[k] = *(const bf16x8*)(smem + abase + 32 * (95 - ii));
        A2[k] = *(const bf16x8*)(smem + abase + 32 * (95 - 64 - ii));
        const u32 pb = (ii & 1) ? (ubase + (16 - (ii - 1) / 2) * 16) : (ubase + 2 * U_QS + (16 - ii / 2) * 16);
        b0[k] = *(const bf16x8*)(smem + pb); b1[k] = *(const bf16x8*)(smem + pb + 32 * 16);
      };
      ld(0); ld(1);
#define CONV_STEP(ii) { if ((ii) + 2 < 32) ld((ii) + 2); SB(); \
        acc0 = mfma32(A1[(ii) % 3], b0[(ii) % 3], acc0); acc1 = mfma32(A1[(ii) % 3], b1[(ii) % 3], acc1); acc1 = mfma32(A2[(ii) % 3], b0[(ii) % 3], acc1); SB(); \
        if ((ii) < 16) LDW(16, (ii) < 16 ? (ii) : 0, (ii) < 16 ? (ii) + 1 : 0, w1, w1p); else LDW(16, (ii) >= 16 ? (ii) - 16 : 0, (ii) >= 16 ? (ii) - 15 : 0, w2, w2p); }
      CONV_STEP(0) CONV_STEP(1) CONV_STEP(2) CONV_STEP(3) CONV_STEP(4) CONV_STEP(5) CONV_STEP(6) CONV_STEP(7)
      CONV_STEP(8) CONV_STEP(9) CONV_STEP(10) CONV_STEP(11) CONV_STEP(12) CONV_STEP(13) CONV_STEP(14) CONV_STEP(15)
      CONV_STEP(16) CONV_STEP(17) CONV_STEP(18) CONV_STEP(19) CONV_STEP(20) CONV_STEP(21) CONV_STEP(22) CONV_STEP(23)
      CONV_STEP(24) CONV_STEP(25) CONV_STEP(26) CONV_STEP(27) CONV_STEP(28) CONV_STEP(29) CONV_STEP(30) CONV_STEP(31)
    }
    BAR();
    {
      f32x16 snd = ih ? acc0 : acc1;
      char* dst = smem + w * 4096 + lane * 16;
#pragma unroll
      for (int q = 0; q < 4; ++q) *(f32x4*)(dst + q * 1024) = (f32x4){snd[4 * q], snd[4 * q + 1], snd[4 * q + 2], snd[4 * q + 3]};
    }
    BAR();
    {
      f32x16 y = ih ? acc1 : acc0;
      const char* src = smem + (w ^ 1) * 4096 + lane * 16;
      u32x2 o[4];
#pragma unroll
      for (int q = 0; q < 4; ++q) {
        const f32x4 p = *(const f32x4*)(src + q * 1024);
        const u32 io = chl * U_CH + q * U_QS + (32 + 32 * ih + n) * 16 + 8 * h;
        const u32x2 uu = *(const u32x2*)(smem + LD_U + io), xx = *(const u32x2*)(smem + LD_X + io);
#pragma unroll
        for (int e = 0; e < 2; ++e) {
          const f32x2 yy = (f32x2){y[4 * q + 2 * e], y[4 * q + 2 * e + 1]} + (f32x2){p[2 * e], p[2 * e + 1]} + (f32x2){bflo(xx[e]), bfhi(xx[e])};
          o[q][e] = pk2v(__builtin_elementwise_fma((f32x2){bflo(uu[e]), bfhi(uu[e])}, (f32x2){dsk, dsk}, yy));
        }
      }
      const u32 hp = WS_HP + (16 * ih + (n >> 1)) * 65536 + ch * 128 + (32 * (n & 1) + 8 * h) * 2;
#pragma unroll
      for (int pr = 0; pr < 2; ++pr) {
        auto r0 = __builtin_amdgcn_permlane32_swap(o[2 * pr][0], o[2 * pr + 1][0], false, false);
        auto r1 = __builtin_amdgcn_permlane32_swap(o[2 * pr][1], o[2 * pr + 1][1], false, false);
        st16(rs, hp + pr * 32, (u32x4){r0[0], r1[0], r0[1], r1[1]});
      }
    }
    SB();
    LDW(64, 0, NW3A, w3, w3p);
    {
      const bf16x8 onesv = {(__bf16)1.f, (__bf16)1.f, (__bf16)1.f, (__bf16)1.f, (__bf16)1.f, (__bf16)1.f, (__bf16)1.f, (__bf16)1.f};
      f32x16 cacc = {};
#pragma unroll
      for (int s = 0; s < 16; ++s) cacc = mfma32(asbf(w1[s]), onesv, cacc);
      if ((lane & 31) == 0) {
        float* dst = (float*)(smem + LD_CS1) + (kh * 2 + nh) * 32 + 4 * h;
#pragma unroll
        for (int q = 0; q < 4; ++q) *(f32x4*)(dst + 8 * q) = (f32x4){cacc[4 * q], cacc[4 * q + 1], cacc[4 * q + 2], cacc[4 * q + 3]};
      }
    }
    SB();
    WAITV_T(NW3A);
    publish(ws, 1, b, tag);
    if (w != 0) LDW(64, NW3A, 40, w3, w3p); else LDW(64, NW3A, 20, w3, w3p);
    wait_all(ws, 1, w, lane, tag);
  }

  const int T = panel * 2 + tto; const int s_out = ct * 4 + nh * 2;
  char* exw = smem + LD_EX + w * 4096 + lane * 16;
  const char* exr = smem + LD_EX + (w ^ 2) * 4096 + lane * 16;
  u32x4 hres[2], xb[2], x1r[2];
  {
    {
      const int half = w >> 1, sub = w & 1;
      const char* sl = ws + WS_HP + (size_t)panel * 65536 + (half * 256 + sub * 32) * 128;
      const u32 vo = (lane >> 3) * 128 + img128_srcoff(lane);
#pragma unroll
      for (int c = 0; c < 3; ++c)
#pragma unroll
        for (int i = 0; i < 4; ++i) glds16(vo, sl + (ROT(4 * c) * 16 + i * 8) * 128, lb + (half * 256 + sub * 32 + c * 64 + i * 8) * 128);
      SB();
      f32x16 acc0 = {}, acc1 = {};
      float sa0 = 0.f, sa1 = 0.f, sq0 = 0.f, sq1 = 0.f;
      const u32 aB0 = kh * 256 * 128 + img128_traddr(lane, tto), aB1 = kh * 256 * 128 + img128_traddr(lane, tto ^ 1);
      const bf16x2 ones = {(__bf16)1.0f, (__bf16)1.0f};
      bf16x8 b0[3], b1[3];
      WAITV(8); RAWBAR();
#pragma unroll
      for (int s = 0; s < 2; ++s) { b0[s] = tr_frag(smem, aB0 + s * 2048, aB0 + s * 2048 + 1024); b1[s] = tr_frag(smem, aB1 + s * 2048, aB1 + s * 2048 + 1024); }
#pragma unroll
      for (int s = 0; s < 16; ++s) {
        if (s + 2 < 16) {
          if (s + 2 == 4) { WAITV(6); RAWBAR(); } else if (s + 2 == 8) { WAITV(4); RAWBAR(); } else if (s + 2 == 12) { WAITV(0); RAWBAR(); }
          b0[(s + 2) % 3] = tr_frag(smem, aB0 + (s + 2) * 2048, aB0 + (s + 2) * 2048 + 1024);
          b1[(s + 2) % 3] = tr_frag(smem, aB1 + (s + 2) * 2048, aB1 + (s + 2) * 2048 + 1024);
        }
        SB();
        acc0 = mfma32(asbf(w1[s]), b0[s % 3], acc0); acc1 = mfma32(asbf(w1[s]), b1[s % 3], acc1);
        SB();
        if (s < 4) glds16(vo, sl + (ROT(12) * 16 + s * 8) * 128, lb + (half * 256 + sub * 32 + 3 * 64 + s * 8) * 128);
        { const bf16x8 st = b0[s % 3];
          const bf16x2 p0 = {st[0], st[1]}, p1 = {st[2], st[3]}, p2 = {st[4], st[5]}, p3 = {st[6], st[7]};
          sa0 = __builtin_amdgcn_fdot2_f32_bf16(p0, ones, sa0, false); sq0 = __builtin_amdgcn_fdot2_f32_bf16(p0, p0, sq0, false);
          sa1 = __builtin_amdgcn_fdot2_f32_bf16(p1, ones, sa1, false); sq1 = __builtin_amdgcn_fdot2_f32_bf16(p1, p1, sq1, false);
          sa0 = __builtin_amdgcn_fdot2_f32_bf16(p2, ones, sa0, false); sq0 = __builtin_amdgcn_fdot2_f32_bf16(p2, p2, sq0, false);
          sa1 = __builtin_amdgcn_fdot2_f32_bf16(p3, ones, sa1, false); sq1 = __builtin_amdgcn_fdot2_f32_bf16(p3, p3, sq1, false); }
        if (s >= 12 && w == 0) { if (s == 12) LDW(64, 20, 25, w3, w3p); else if (s == 13) LDW(64, 25, 30, w3, w3p); else if (s == 14) LDW(64, 30, 35, w3, w3p); else LDW(64, 35, 40, w3, w3p); }
        SB();
      }
      float ssum = sa0 + sa1, ssq = sq0 + sq1;
#pragma unroll
      for (int q = 0; q < 4; ++q) *(f32x4*)(exw + q * 1024) = (f32x4){acc1[4 * q], acc1[4 * q + 1], acc1[4 * q + 2], acc1[4 * q + 3]};
      ssum = swap32_add(ssum); ssq = swap32_add(ssq);
      if (lane < 32) *(f32x2*)(smem + LD_SM + ((kh * 2 + tto) * 32 + lane) * 8) = (f32x2){ssum, ssq};
      BAR();
      f32x16 z = acc0;
#pragma unroll
      for (int q = 0; q < 4; ++q) { f32x4 p = *(const f32x4*)(exr + q * 1024);
#pragma unroll
        for (int j = 0; j < 4; ++j) z[4 * q + j] += p[j]; }
      f32x4 cs[4];
#pragma unroll
      for (int q = 0; q < 4; ++q) cs[q] = *(const f32x4*)(smem + LD_CS1 + ((0 * 2 + nh) * 32 + 8 * q + 4 * h) * 4) + *(const f32x4*)(smem + LD_CS1 + ((1 * 2 + nh) * 32 + 8 * q + 4 * h) * 4);
      f32x2 s0 = *(const f32x2*)(smem + LD_SM + ((0 * 2 + tto) * 32 + (lane & 31)) * 8);
      f32x2 s1 = *(const f32x2*)(smem + LD_SM + ((1 * 2 + tto) * 32 + (lane & 31)) * 8);
      const float mean = (s0[0] + s1[0]) * (1.f / D);
      const float var = fmaxf((s0[1] + s1[1]) * (1.f / D) - mean * mean, 0.f);
      const float rs1 = __builtin_amdgcn_rsqf(var + 1e-5f);
      const float var2 = var * rs1 * rs1;
      const float rsc = rs1 * __builtin_amdgcn_rsqf(var2 + 1e-5f);
      const u32 ah = img128_traddr(lane, tto);
      const f32x2 R1 = {rs1, rs1}, MR = {-mean * rs1, -mean * rs1};
#pragma unroll
      for (int sp = 0; sp < 2; ++sp) {
        const int s = s_out + sp;
        const u32 apos = ah + ((s & 16) + (((s & 15) - rot) & 15)) * 2048;
        bf16x8 hf = tr_frag(smem, apos, apos + 1024);
        u32x4 hv = __builtin_bit_cast(u32x4, hf);
        u32x4 o;
#pragma unroll
        for (int j = 0; j < 4; ++j) {
          int r = 8 * sp + 2 * j;
          o[j] = gelu_ln2(z[r], z[r + 1], cs[r >> 2][r & 3], cs[(r + 1) >> 2][(r + 1) & 3], mean, rsc);
          xb[sp][j] = pk2v(__builtin_elementwise_fma((f32x2){bflo(hv[j]), bfhi(hv[j])}, R1, MR));
        }
        hres[sp] = hv;
        st16(rs, WS_H1 + 32 * 1024 + (T * 32 + s) * 1024 + lane * 16, o);
      }
    }
    SB();
    LDW(64, 40, 46, w3, w3p);
    WAITV(6);
    publish(ws, 2, me, tag);
    if (w != 0) LDW(64, 46, 64, w3, w3p); else LDW(64, 46, 52, w3, w3p);
  }
  {
    {
      int ix = -1;
      if (lane < 8) ix = panel * 8 + lane;
      else if (lane < 12) { if (panel > 0) ix = (panel - 1) * 8 + ((lane & 1) | ((lane & 2) << 1)); }
      else if (lane < 16) { if (panel < 31) ix = (panel + 1) * 8 + 2 + ((lane & 1) | ((lane & 2) << 1)); }
      wait_flags(ws, 2, ix, w, tag);
    }
    f32x16 c0a = {}, c1a = {};
    {
      const int half = w >> 1, tt = w & 1;
      u32 vo[2];
#pragma unroll
      for (int gg = 0; gg < 2; ++gg) {
        int sh = half ? (gg ? 2 : -2) : (gg ? 1 : -1);
        int tok = tt * 32 + (lane & 31) + sh + 32;
        vo[gg] = (tok >> 5) * 32 * 1024 + ((tok & 31) + 32 * (lane >> 5)) * 16;
      }
      const char* sl = ws + WS_H1 + (size_t)(panel * 2) * 32 * 1024 + (size_t)half * 16 * 1024;
      u32 voc[4];
#pragma unroll
      for (int c = 0; c < 4; ++c) voc[c] = (ROT(4 * c) >> 3) ? vo[1] : vo[0];
#pragma unroll
      for (int s = 0; s < 12; ++s) glds16(voc[s >> 2], sl + ROT(s) * 1024, lb + ((half * 16 + s) * 2 + tt) * 1024);
      SB();
      const u32 aB = kh * 32 * 1024 + lane * 16 + tto * 1024, aBo = aB ^ 1024;
      bf16x8 p0[3], p1[3];
      WAITV(8); RAWBAR();
#pragma unroll
      for (int s = 0; s < 2; ++s) { p0[s] = *(const bf16x8*)(smem + aB + s * 2048); p1[s] = *(const bf16x8*)(smem + aBo + s * 2048); }
#pragma unroll
      for (int s = 0; s < 16; ++s) {
        if (s + 2 < 16) {
          if (s + 2 == 4) { WAITV(6); RAWBAR(); } else if (s + 2 == 8) { WAITV(4); RAWBAR(); } else if (s + 2 == 12) { WAITV(0); RAWBAR(); }
          p0[(s + 2) % 3] = *(const bf16x8*)(smem + aB + (s + 2) * 2048); p1[(s + 2) % 3] = *(const bf16x8*)(smem + aBo + (s + 2) * 2048);
        }
        SB();
        c0a = mfma32(asbf(w2[s]), p0[s % 3], c0a); c1a = mfma32(asbf(w2[s]), p1[s % 3], c1a);
        SB();
        if (s < 4) glds16(voc[3], sl + ROT(12 + s) * 1024, lb + ((half * 16 + 12 + s) * 2 + tt) * 1024);
        if (s >= 12 && w == 0) { if (s == 12) LDW(64, 52, 55, w3, w3p); else if (s == 13) LDW(64, 55, 58, w3, w3p); else if (s == 14) LDW(64, 58, 61, w3, w3p); else LDW(64, 61, 64, w3, w3p); }
        SB();
      }
    }
#pragma unroll
    for (int q = 0; q < 4; ++q) *(f32x4*)(exw + q * 1024) = (f32x4){c1a[4 * q], c1a[4 * q + 1], c1a[4 * q + 2], c1a[4 * q + 3]};
    BAR();
    {
      f32x16 z = c0a;
#pragma unroll
      for (int q = 0; q < 4; ++q) { f32x4 p = *(const f32x4*)(exr + q * 1024);
#pragma unroll
        for (int j = 0; j < 4; ++j) z[4 * q + j] += p[j]; }
      f32x2 ssum2 = {0.f, 0.f}, ssq2 = {0.f, 0.f};
#pragma unroll
      for (int sp = 0; sp < 2; ++sp) {
        u32x4 o;
#pragma unroll
        for (int j = 0; j < 4; ++j) {
          int r = 8 * sp + 2 * j;
          u32 p = pk2v((f32x2){bflo(xb[sp][j]), bfhi(xb[sp][j])} + (f32x2){z[r], z[r + 1]});
          o[j] = p; const f32x2 a = {bflo(p), bfhi(p)};
          ssum2 += a; ssq2 = __builtin_elementwise_fma(a, a, ssq2);
        }
        x1r[sp] = o;
        st16(rs, WS_X1 + (T * 32 + s_out + sp) * 1024 + lane * 16, o);
      }
      float ssum = swap32_add(ssum2[0] + ssum2[1]), ssq = swap32_add(ssq2[0] + ssq2[1]);
      if (lane < 32) __builtin_amdgcn_raw_buffer_store_b64((u32x2){__float_as_uint(ssum), __float_as_uint(ssq)}, rs,
                                                           WS_ST + ((T * 32 + lane) * 16 + ct * 2 + nh) * 8, 0, 16);
    }
    SB();
    LDW4(0, 12);
    WAITV(12);
    publish(ws, 3, me, tag);
  }
  const char* slh = ws + WS_HH + ((size_t)(panel * 2 + (w & 1)) * 112 + (w >> 1) * 56) * 1024;
  {
    const bf16x8 onesv = {(__bf16)1.f, (__bf16)1.f, (__bf16)1.f, (__bf16)1.f, (__bf16)1.f, (__bf16)1.f, (__bf16)1.f, (__bf16)1.f};
    f32x4 ca[4] = {};
#pragma unroll
    for (int s = 0; s < 16; ++s)
#pragma unroll
      for (int i = 0; i < 4; ++i) ca[i] = mfma16(asbf(w3[i * 16 + s]), onesv, ca[i]);
    SB();
#pragma unroll
    for (int i = 18; i < 34; ++i) glds16(lane * 16, ws + w4p + JL(i) * 1024, lb + 65536 + w * 16384 + (i - 18) * 1024);
    SB();
    wait_flags(ws, 3, lane < 8 ? panel * 8 + lane : -1, w, tag);
    u32x4 stv[2];
    const char* slx = ws + WS_X1 + (size_t)(panel * 2) * 32 * 1024;
    auto stg3 = [&](int c, int i) { const int s16 = 8 * c + 2 * w + (i >> 1), tt = i & 1;
      glds16(lane * 16, slx + (tt * 32 + 2 * ROT(4 * c) + 2 * w + (i >> 1)) * 1024, lb + (s16 * 2 + tt) * 1024); };
#pragma unroll
    for (int c = 0; c < 3; ++c)
#pragma unroll
      for (int i = 0; i < 4; ++i) stg3(c, i);
    SB();
    f32x4 acc[14];
#pragma unroll
    for (int i = 0; i < 14; ++i) acc[i] = (f32x4){0.f, 0.f, 0.f, 0.f};
    const int g = lane >> 4;
    const u32 aBa = ((g >> 1) * 2) * 1024 + ((lane & 15) + 32 * (g & 1)) * 16 + (w & 1) * 1024, aBb = aBa ^ 1024;
    bf16x8 bq[2][4];
    auto ldb = [&](int s, int k) {
#pragma unroll
      for (int tk = 0; tk < 4; ++tk) bq[k][tk] = *(const bf16x8*)(smem + ((tk >> 1) ? aBb : aBa) + s * 4096 + (tk & 1) * 256);
    };
    WAITV(8); RAWBAR();
    ldb(0, 0);
#pragma unroll
    for (int s = 0; s < 16; ++s) {
      if (s + 1 < 16) { if (s + 1 == 4) { WAITV(7); RAWBAR(); } else if (s + 1 == 8) { WAITV(8); RAWBAR(); } else if (s + 1 == 12) { WAITV(8); BAR(); } }
      ldb((s + 1) & 15, (s + 1) & 1);
      SB();
#pragma unroll
      for (int i = 0; i < 2; ++i)
#pragma unroll
        for (int tk = 0; tk < 4; ++tk) acc[i * 4 + tk] = mfma16(asbf(w3[i * 16 + s]), bq[s & 1][tk], acc[i * 4 + tk]);
      SB();
      if (s < 4) stg3(3, s);
      else if (s == 4) {
#pragma unroll
        for (int q = 0; q < 2; ++q) stv[q] = ld16(rs, (panel * 64 + w * 16 + (lane & 15)) * 128 + (lane >> 4) * 32 + q * 16, WS_ST);
      } else if (s < 11) {
        if (s == 5) LDW4(12, 13); else if (s == 6) LDW4(13, 14); else if (s == 7) LDW4(14, 15); else if (s == 8) LDW4(15, 16); else if (s == 9) LDW4(16, 17); else LDW4(17, 18);
        if (s == 10) {
          float a = (__uint_as_float(stv[0][0]) + __uint_as_float(stv[0][2])) + (__uint_as_float(stv[1][0]) + __uint_as_float(stv[1][2]));
          float q2 = (__uint_as_float(stv[0][1]) + __uint_as_float(stv[0][3])) + (__uint_as_float(stv[1][1]) + __uint_as_float(stv[1][3]));
          a = swap32_add(swap16_add(a)); q2 = swap32_add(swap16_add(q2));
          const float mn = a * (1.f / D);
          if (lane < 16) *(f32x2*)(smem + LD_SM + (w * 16 + lane) * 8) = (f32x2){mn, __builtin_amdgcn_rsqf(fmaxf(q2 * (1.f / D) - mn * mn, 0.f) + 1e-5f)};
        }
      }
      SB();
    }
#pragma unroll
    for (int i = 18; i < 34; ++i) w4[i] = *(const u32x4*)(smem + 65536 + w * 16384 + (i - 18) * 1024 + lane * 16);
    float mean[4], rsc[4];
#pragma unroll
    for (int tk = 0; tk < 4; ++tk) {
      const f32x2 mr = *(const f32x2*)(smem + LD_SM + ((tk ^ (2 * (w & 1))) * 16 + (lane & 15)) * 8);
      mean[tk] = mr[0]; rsc[tk] = mr[1];
    }
    auto gel = [&](int ti, int slot, int tk) -> u32x2 {
      const f32x4 c = ca[slot]; const float m = mean[tk], r = rsc[tk];
      return (u32x2){gelu_ln2(acc[ti][0], acc[ti][1], c[0], c[1], m, r), gelu_ln2(acc[ti][2], acc[ti][3], c[2], c[3], m, r)};
    };
    const int tkx = 2 * (w & 1);
    auto stpair = [&](u32x2 A, u32x2 B, int jA, int tkA, int jB, int tkB) {
      auto r0 = __builtin_amdgcn_permlane32_swap(A[0], B[0], false, false);
      auto r1 = __builtin_amdgcn_permlane32_swap(A[1], B[1], false, false);
      const int j = lane < 32 ? jA : jB, tk = (lane < 32 ? tkA : tkB) ^ tkx;
      st16(rs, WS_HH + ((panel * 2 + (tk >> 1)) * 112 + j) * 1024 + (16 * (tk & 1) + (lane & 15) + 32 * (g & 1)) * 16, (u32x4){r0[0], r1[0], r0[1], r1[1]});
    };
    const int j0 = ct * 14 + 3 * w, js = ct * 14 + 12 + (w >> 1);
    stpair(gel(0, 0, 0), gel(4, 1, 0), j0, 0, j0 + 1, 0); SB(); LDW4(34, 37);
    stpair(gel(1, 0, 1), gel(5, 1, 1), j0, 1, j0 + 1, 1); SB(); LDW4(37, 40);
    stpair(gel(2, 0, 2), gel(6, 1, 2), j0, 2, j0 + 1, 2); SB(); LDW4(40, 43);
    stpair(gel(3, 0, 3), gel(7, 1, 3), j0, 3, j0 + 1, 3); SB(); LDW4(43, 45);
#pragma unroll
    for (int s = 0; s < 16; ++s) {
      if (s + 1 < 16) ldb(s + 1, (s + 1) & 1);
      SB();
#pragma unroll
      for (int tk = 0; tk < 4; ++tk) acc[8 + tk] = mfma16(asbf(w3[32 + s]), bq[s & 1][tk], acc[8 + tk]);
      acc[12] = mfma16(asbf(w3[48 + s]), bq[s & 1][0], acc[12]); acc[13] = mfma16(asbf(w3[48 + s]), bq[s & 1][1], acc[13]);
      SB();
      if (s < 5) { if (s == 0) LDW4(45, 46); else if (s == 1) LDW4(46, 47); else if (s == 2) LDW4(47, 48); else if (s == 3) LDW4(48, 49); else LDW4(49, 50); }
      else if (s == 5) { WAITV(7); publish(ws, 4, me, tag); LDW4(50, 51); }
      else if (s < 11) { if (s == 6) LDW4(51, 52); else if (s == 7) LDW4(52, 53); else if (s == 8) LDW4(53, 54); else if (s == 9) LDW4(54, 55); else LDW4(55, 56); }
      SB();
    }
    wait_flags(ws, 4, lane < 8 ? panel * 8 + lane : -1, w, tag);
#define STAGE4(c, s4) glds16(lane * 16, slh + JL((c) * 4 + (s4)) * 1024, lb + ((c) & 7) * 16384 + (((w >> 1) * 4 + (s4)) * 2 + (w & 1)) * 1024)
#pragma unroll
    for (int c = 0; c < 3; ++c)
#pragma unroll
      for (int s4 = 0; s4 < 4; ++s4) STAGE4(c, s4);
    SB();
    stpair(gel(8, 2, 0), gel(9, 2, 1), j0 + 2, 0, j0 + 2, 1);
    stpair(gel(10, 2, 2), gel(11, 2, 3), j0 + 2, 2, j0 + 2, 3);
    stpair(gel(12, 3, 0), gel(13, 3, 1), js, 0, js, 1);
    SB();
  }
  {
    f32x16 c0a = {}, c1a = {};
    const u32 aB = kh * 8 * 1024 + lane * 16 + tto * 1024, aBo = aB ^ 1024;
    bf16x8 p0[3], p1[3];
    WAITV(11);
    RAWBAR();
#pragma unroll
    for (int s = 0; s < 2; ++s) { p0[s] = *(const bf16x8*)(smem + aB + s * 2048); p1[s] = *(const bf16x8*)(smem + aBo + s * 2048); }
#pragma unroll
    for (int s = 0; s < 56; ++s) {
      if (s + 2 < 56) {
        const int c = (s + 2) >> 2;
        if (((s + 2) & 3) == 0) {
          if (c == 1) WAITV(9); else if (c + 2 < 14) WAITV(6); else if (c + 2 == 14) WAITV(4); else WAITV(0);
          if (c == 2) publish(ws, 5, me, tag);
          else if (c == 5) wait_flags(ws, 5, lane < 8 ? panel * 8 + lane : -1, w, tag);
          else RAWBAR();
        }
        const u32 sa = (c & 7) * 16384 + ((s + 2) & 3) * 2048;
        p0[(s + 2) % 3] = *(const bf16x8*)(smem + sa + aB); p1[(s + 2) % 3] = *(const bf16x8*)(smem + sa + aBo);
      }
      SB();
      c0a = mfma32(asbf(w4[s]), p0[s % 3], c0a); c1a = mfma32(asbf(w4[s]), p1[s % 3], c1a);
      SB();
      if ((s >> 2) + 3 < 14) STAGE4((s >> 2) + 3, s & 3);
      SB();
    }
#pragma unroll
    for (int q = 0; q < 4; ++q) *(f32x4*)(exw + q * 1024) = (f32x4){c1a[4 * q], c1a[4 * q + 1], c1a[4 * q + 2], c1a[4 * q + 3]};
    BAR();
    {
      f32x16 z = c0a;
#pragma unroll
      for (int q = 0; q < 4; ++q) { f32x4 p = *(const f32x4*)(exr + q * 1024);
#pragma unroll
        for (int j = 0; j < 4; ++j) z[4 * q + j] += p[j]; }
      u32x2 o[4];
#pragma unroll
      for (int qq = 0; qq < 4; ++qq) {
        const int sp = qq >> 1, jj = (qq & 1) * 2;
#pragma unroll
        for (int e = 0; e < 2; ++e) {
          const u32 xp = x1r[sp][jj + e], hp = hres[sp][jj + e];
          o[qq][e] = pk2v((f32x2){bflo(xp), bfhi(xp)} + (f32x2){bflo(hp), bfhi(hp)} + (f32x2){z[4 * qq + 2 * e], z[4 * qq + 2 * e + 1]});
        }
      }
      char* op = (char*)out + ((size_t)(T * 32 + (lane & 31)) * D + ct * 64 + nh * 32) * 2 + h * 16;
#pragma unroll
      for (int pr = 0; pr < 2; ++pr) {
        auto r0 = __builtin_amdgcn_permlane32_swap(o[2 * pr][0], o[2 * pr + 1][0], false, false);
        auto r1 = __builtin_amdgcn_permlane32_swap(o[2 * pr][1], o[2 * pr + 1][1], false, false);
        *(u32x4*)(op + pr * 32) = (u32x4){r0[0], r1[0], r0[1], r1[1]};
      }
    }
  }
  asm volatile("km_code_end:");
}

extern "C" void kernel_launch(void* const* d_in, const int* in_sizes, int n_in, void* d_out, int out_size, void* d_ws, size_t ws_size,
                              hipStream_t stream) {
  (void)hipFuncSetAttribute((const void*)kmain, hipFuncAttributeMaxDynamicSharedMemorySize, LDS_BYTES);
  kmain<<<256, 256, LDS_BYTES, stream>>>((const u16*)d_in[0], (const u16*)d_in[1], (const u16*)d_in[2], (const u16*)d_in[3], (const u16*)d_in[4],
                                         (const u16*)d_in[5], (const u16*)d_in[6], (const u16*)d_in[8], (const u16*)d_in[9], (const u16*)d_in[10],
                                         (const u16*)d_in[11], (char*)d_ws, (u16*)d_out);
}
```
